```python
import torch

def _softmax(x, axis=-1):
    return torch.softmax(x, dim=axis)
def _gelu(x):
    return torch.nn.functional.gelu(x, approximate="tanh")
def _silu(x):
    return torch.nn.functional.silu(x)
def _sigmoid(x):
    return torch.sigmoid(x)
def _layernorm(x, axis=-1, eps=1e-5):
    m = x.mean(dim=axis, keepdim=True)
    v = x.var(dim=axis, keepdim=True, unbiased=False)
    return (x - m) / torch.sqrt(v + eps)
def _scatter_add(tgt, idx, src, axis=0):
    return tgt.clone().index_add_(axis, idx, src)
def _segment_sum(src, seg, n):
    out = torch.zeros((n,) + src.shape[1:], dtype=src.dtype)
    return out.index_add_(0, seg, src)
def _pad(x, pw):
    flat = tuple(v for pair in reversed(pw) for v in pair)
    return torch.nn.functional.pad(x, flat)

kernel_name = 'interleave_layers__sliding_block__geglu_ffn__prompt'

def setup_inputs():

    _w = (lambda a: (lambda t: t.to(torch.bfloat16) if t.is_floating_point() else t)(torch.as_tensor(a)))
    def _A_setup():
        B, S, D = 1, 2048, 448
        return {
            "x": _w((lambda *s: torch.randn(*s, dtype=torch.bfloat16) * 0.05)(B, S, D)),
            "wqkv": _w((lambda *s: torch.randn(*s, dtype=torch.bfloat16) * 0.05)(D, 3 * D)),
            "wo": _w((lambda *s: torch.randn(*s, dtype=torch.bfloat16) * 0.05)(D, D)),
        }
    _A_in = _A_setup()

    def _B_setup():
        B, S, D, F = 1, 2048, 448, 1792
        return {
            "x": _w((lambda *s: torch.randn(*s, dtype=torch.bfloat16) * 0.05)(B, S, D)),
            "w_gate": _w((lambda *s: torch.randn(*s, dtype=torch.bfloat16) * 0.05)(D, F)),
            "w_up": _w((lambda *s: torch.randn(*s, dtype=torch.bfloat16) * 0.05)(D, F)),
            "w_down": _w((lambda *s: torch.randn(*s, dtype=torch.bfloat16) * 0.05)(F, D)),
        }
    _B_in = _B_setup()

    return {
        "A_x": _A_in["x"],
        "A_wqkv": _A_in["wqkv"],
        "A_wo": _A_in["wo"],
        "B_x": _B_in["x"],
        "B_w_gate": _B_in["w_gate"],
        "B_w_up": _B_in["w_up"],
        "B_w_down": _B_in["w_down"],
    }

def reference(A_x, A_wqkv, A_wo, B_x, B_w_gate, B_w_up, B_w_down):
    def _A_ref(x, wqkv, wo):
        B, S, D = x.shape
        H = 16; Dh = D // H; W = 256
        qkv = (lambda x, *d: x.permute(*d))((x @ wqkv).reshape(B, S, 3, H, Dh), 2, 0, 3, 1, 4)
        q, k, v = qkv[0], qkv[1], qkv[2]
        scores = torch.einsum("bhqd,bhkd->bhqk", q, k) * (Dh ** -0.5)
        idx = torch.arange(S)
        mask = (idx[:, None] - idx[None, :] >= 0) & (idx[:, None] - idx[None, :] < W)
        scores = torch.where(mask[None, None], scores, -1e9)
        p = _softmax(scores, axis=-1)
        o = torch.einsum("bhqk,bhkd->bhqd", p, v)
        return (lambda x, *d: x.permute(*d))(o, 0, 2, 1, 3).reshape(B, S, D) @ wo

    def _B_ref(x, w_gate, w_up, w_down):
        return (_gelu(x @ w_gate) * (x @ w_up)) @ w_down

    x = A_x
    x = x + _A_ref(_layernorm(x, axis=-1), A_wqkv, A_wo)
    x = x + _B_ref(_layernorm(x, axis=-1), B_w_gate, B_w_up, B_w_down)
    x = x + _A_ref(_layernorm(x, axis=-1), A_wqkv, A_wo)
    x = x + _B_ref(_layernorm(x, axis=-1), B_w_gate, B_w_up, B_w_down)
    return x
```

```cpp
#include <hip/hip_runtime.h>
#include <hip/hip_bf16.h>
#include <stdint.h>

typedef short bf16x8 __attribute__((ext_vector_type(8)));
typedef float f32x4 __attribute__((ext_vector_type(4)));
typedef unsigned u32x4 __attribute__((ext_vector_type(4)));
typedef unsigned u32x2 __attribute__((ext_vector_type(2)));
typedef __bf16 bf16x2_t __attribute__((ext_vector_type(2)));
#define GAS __attribute__((address_space(1)))
#define LAS __attribute__((address_space(3)))

constexpr int S_ = 2048, D_ = 448, NH = 16, DH = 28, F_ = 1792;
constexpr int NT = 512;

constexpr size_t KB = 1024;
constexpr size_t OFF_WQKV = 64 * KB;
constexpr size_t OFF_WO   = OFF_WQKV + 16 * 6 * 14 * KB;
constexpr size_t OFF_WGU  = OFF_WO + 28 * 16 * KB;
constexpr size_t OFF_WD   = OFF_WGU + 112 * 28 * KB;
constexpr size_t OFF_CS   = OFF_WD + 7 * 56 * 4 * KB;
constexpr size_t OFF_X    = OFF_CS + 32 * KB;
constexpr size_t X_BYTES  = 128 * 28 * 512;
constexpr size_t OFF_Q    = OFF_X + 4 * X_BYTES;
constexpr size_t QKV_BYTES = 16 * 128 * KB;
constexpr size_t OFF_K    = OFF_Q + QKV_BYTES;
constexpr size_t OFF_V    = OFF_K + QKV_BYTES;
constexpr size_t OFF_O    = OFF_V + QKV_BYTES;
constexpr size_t OFF_H    = OFF_O + QKV_BYTES;

__device__ __forceinline__ f32x4 mfma(u32x4 a, u32x4 b, f32x4 c) {
  return __builtin_amdgcn_mfma_f32_16x16x32_bf16(__builtin_bit_cast(bf16x8, a), __builtin_bit_cast(bf16x8, b), c, 0, 0, 0);
}
__device__ __forceinline__ unsigned pk(float a, float b) {
  bf16x2_t v = {(__bf16)a, (__bf16)b};
  return __builtin_bit_cast(unsigned, v);
}
__device__ __forceinline__ float bflo(unsigned u) { return __builtin_bit_cast(float, u << 16); }
__device__ __forceinline__ float bfhi(unsigned u) { return __builtin_bit_cast(float, u & 0xffff0000u); }
__device__ __forceinline__ void dma(const char* g, char* l, int nkb, int wave, int lane) {
  for (int i = wave; i < nkb; i += 8)
    __builtin_amdgcn_global_load_lds((const GAS void*)(g + i * 1024 + lane * 16), (LAS void*)(l + i * 1024), 16, 0, 0);
}
__device__ __forceinline__ u32x4 ldg16(const char* p) { return *(const u32x4*)p; }
__device__ __forceinline__ u32x4 lds16(const char* p) { return *(const u32x4*)p; }
#define WAIT_VM0() asm volatile("s_waitcnt vmcnt(0)" ::: "memory")

__device__ __forceinline__ void cmap(int b, int& c, int& m) { c = (b & 7) * 2 + ((b >> 3) & 1); m = b >> 4; }

__device__ __forceinline__ int kk_perm1(int g, int e) { return 4 * g + (e & 3) + 16 * (e >> 2); }
__device__ __forceinline__ int kk_perm2(int g, int e) { return 16 * (g >> 1) + 4 * (g & 1) + (e & 3) + 8 * (e >> 2); }

__global__ __launch_bounds__(NT) void k_prep(const uint16_t* __restrict__ x, const uint16_t* __restrict__ wqkv,
                                             const uint16_t* __restrict__ wo, const uint16_t* __restrict__ wg,
                                             const uint16_t* __restrict__ wu, const uint16_t* __restrict__ wd, char* ws) {
  __shared__ __attribute__((aligned(16))) char sm[448 * 72 + 8 * 32 * 4];
  const int b = blockIdx.x, tid = threadIdx.x, lane = tid & 63, wave = tid >> 6;
  if (b >= 230) {
    char* xf = ws + OFF_X;
    for (int ch = (b - 230) * NT + tid; ch < 128 * 896; ch += 26 * NT) {
      int tile = ch / 896, r = ch % 896, J = r >> 5, lp = r & 31, i = lp & 15, gp = lp >> 4;
      const uint16_t* src = x + (size_t)(tile * 16 + i) * D_ + 16 * J + 4 * gp;
      u32x2 a = *(const u32x2*)src, c = *(const u32x2*)(src + 8);
      *(u32x4*)(xf + (size_t)ch * 16) = u32x4{a.x, a.y, c.x, c.y};
    }
    return;
  }
  const uint16_t* src; int ld, ncols = 32, type; char* dst; float* cs = nullptr; int s_stride, nt_stride;
  if (b < 48) {
    int h = b / 3, p = b % 3; type = 0;
    src = wqkv + p * D_ + DH * h; ld = 3 * D_; ncols = DH;
    dst = ws + OFF_WQKV + (size_t)(h * 3 + p) * 28 * KB; s_stride = 1024; nt_stride = 14 * 1024;
    cs = (float*)(ws + OFF_CS) + (h * 3 + p) * 32;
  } else if (b < 62) {
    int j = b - 48; type = 1;
    src = wo + 32 * j; ld = D_;
    dst = ws + OFF_WO + (size_t)(2 * j) * 16 * KB; s_stride = 1024; nt_stride = 16 * 1024;
  } else if (b < 174) {
    int j = (b - 62) >> 1, u = (b - 62) & 1; type = 0;
    src = (u ? wu : wg) + 32 * j; ld = F_;
    dst = ws + OFF_WGU + (size_t)(2 * j) * 28 * KB + u * 1024; s_stride = 2048; nt_stride = 28 * 1024;
    cs = (float*)(ws + OFF_CS) + 48 * 32 + u * F_ + 32 * j;
  } else {
    int j = (b - 174) >> 2, q = (b - 174) & 3; type = 0;
    src = wd + (size_t)q * 448 * D_ + 32 * j; ld = D_;
    dst = ws + OFF_WD + ((size_t)(j >> 1) * 56 + q * 14) * 4 * KB + (j & 1) * 2 * KB; s_stride = 4096; nt_stride = 1024;
  }
  for (int u = 0; u < 7; ++u) {
    int ch = u * NT + tid, row = ch >> 3, c4 = ch & 7;
    u32x2 v = {0u, 0u};
    if (c4 * 4 < ncols) v = *(const u32x2*)(src + (size_t)row * ld + c4 * 4);
    *(u32x2*)(sm + row * 72 + c4 * 8) = v;
  }
  __syncthreads();
  const int i = lane & 15, g = lane >> 4;
  const int nfr = type == 1 ? 32 : 28;
  for (int f = wave; f < nfr; f += 8) {
    int s = f >> 1, nt = f & 1;
    unsigned e16[8];
#pragma unroll
    for (int e = 0; e < 8; ++e) {
      int row; bool ok = true;
      if (type == 1) { int kk = kk_perm1(g, e); ok = kk < DH; row = DH * s + (ok ? kk : 0); }
      else row = 32 * s + kk_perm2(g, e);
      unsigned v = *(const uint16_t*)(sm + row * 72 + (16 * nt + i) * 2);
      e16[e] = ok ? v : 0u;
    }
    u32x4 o = {e16[0] | (e16[1] << 16), e16[2] | (e16[3] << 16), e16[4] | (e16[5] << 16), e16[6] | (e16[7] << 16)};
    *(u32x4*)(dst + (size_t)s * s_stride + (size_t)nt * nt_stride + lane * 16) = o;
  }
  if (cs) {
    int col = tid & 31, rg = tid >> 5;
    float a = 0.f;
    for (int r = 0; r < 28; ++r) a += bflo(*(const uint16_t*)(sm + (rg * 28 + r) * 72 + col * 2));
    a += __shfl_xor(a, 32);
    float* red = (float*)(sm + 448 * 72);
    if (lane < 32) red[wave * 32 + col] = a;
    __syncthreads();
    if (tid < 32) {
      float t = 0.f;
      for (int w = 0; w < 8; ++w) t += red[w * 32 + tid];
      cs[tid] = t;
    }
  }
}

template <int NTL>
__device__ __forceinline__ void ln_stats(const char* xs, float2* st, int lane) {
  const u32x4 ones = {0x3f803f80u, 0x3f803f80u, 0x3f803f80u, 0x3f803f80u};
  f32x4 a1[NTL], a2[NTL];
#pragma unroll
  for (int t = 0; t < NTL; ++t) { a1[t] = f32x4{0, 0, 0, 0}; a2[t] = f32x4{0, 0, 0, 0}; }
#pragma unroll
  for (int s = 0; s < 14; ++s)
#pragma unroll
    for (int t = 0; t < NTL; ++t) {
      u32x4 xf = lds16(xs + (t * 14 + s) * 1024 + lane * 16);
      a1[t] = mfma(ones, xf, a1[t]);
      a2[t] = mfma(xf, xf, a2[t]);
    }
  const int i = lane & 15, g = lane >> 4;
#pragma unroll
  for (int t = 0; t < NTL; ++t) {
    int r = i & 3;
    float q = r == 0 ? a2[t][0] : r == 1 ? a2[t][1] : r == 2 ? a2[t][2] : a2[t][3];
    float mean = a1[t][0] * (1.f / D_);
    float var = fmaxf(q * (1.f / D_) - mean * mean, 0.f);
    if (g == (i >> 2)) st[t * 16 + i] = float2{mean, __builtin_amdgcn_rsqf(var + 1e-5f)};
  }
}

__global__ __launch_bounds__(NT, 2) void k_qkv(char* ws, int xin) {
  extern __shared__ __attribute__((aligned(16))) char sm[];
  int c, h; cmap(blockIdx.x, c, h);
  const int tid = threadIdx.x, lane = tid & 63, wave = __builtin_amdgcn_readfirstlane(tid >> 6);
  const int i = lane & 15, g = lane >> 4;
  const char* xf = ws + OFF_X + xin * X_BYTES;
  for (int X = 0; X < 2; ++X) dma(xf + (size_t)(X * 64 + 4 * c) * 14 * KB, sm + X * 56 * 1024, 56, wave, lane);
  float2* st = (float2*)(sm + 112 * 1024);
  const int p = wave >> 1, mh = wave & 1;
  u32x4 w[2][14];
  float csv[2][4];
  if (wave < 6) {
    const char* wp = ws + OFF_WQKV + (size_t)(h * 3 + p) * 28 * KB + lane * 16;
#pragma unroll
    for (int nt = 0; nt < 2; ++nt)
#pragma unroll
      for (int s = 0; s < 14; ++s) w[nt][s] = ldg16(wp + (nt * 14 + s) * 1024);
    const float* cs = (const float*)(ws + OFF_CS) + (h * 3 + p) * 32;
#pragma unroll
    for (int nt = 0; nt < 2; ++nt)
#pragma unroll
      for (int r = 0; r < 4; ++r) csv[nt][r] = p < 2 ? cs[16 * nt + 4 * g + r] : cs[16 * nt + i];
  }
  WAIT_VM0();
  __syncthreads();
  for (int X = 0; X < 2; ++X) {
    const char* xs = sm + X * 56 * 1024;
    const int T0 = X * 64 + 4 * c;
    f32x4 acc[2][2] = {};
    if (wave < 4) {
#pragma unroll
      for (int s = 0; s < 14; ++s)
#pragma unroll
        for (int t = 0; t < 2; ++t) {
          u32x4 xv = lds16(xs + ((2 * mh + t) * 14 + s) * 1024 + lane * 16);
#pragma unroll
          for (int nt = 0; nt < 2; ++nt) acc[t][nt] = mfma(w[nt][s], xv, acc[t][nt]);
        }
    } else if (wave < 6) {
#pragma unroll
      for (int s = 0; s < 14; ++s)
#pragma unroll
        for (int t = 0; t < 2; ++t) {
          u32x4 xv = lds16(xs + ((2 * mh + t) * 14 + s) * 1024 + lane * 16);
#pragma unroll
          for (int nt = 0; nt < 2; ++nt) acc[t][nt] = mfma(xv, w[nt][s], acc[t][nt]);
        }
    } else {
      ln_stats<2>(xs + (wave - 6) * 28 * 1024, st + X * 64 + (wave - 6) * 32, lane);
    }
    __syncthreads();
    if (wave < 4) {
      const float sc = p == 0 ? 0.18898223650461361f * 1.4426950408889634f : 1.f;
      char* dst = ws + (p == 0 ? OFF_Q : OFF_K) + ((size_t)h * 128 + T0 + 2 * mh) * KB + lane * 16;
#pragma unroll
      for (int t = 0; t < 2; ++t) {
        float2 ms = st[X * 64 + (2 * mh + t) * 16 + i];
        float rs = ms.y * sc;
        float v[8];
#pragma unroll
        for (int nt = 0; nt < 2; ++nt)
#pragma unroll
          for (int r = 0; r < 4; ++r) v[nt * 4 + r] = (acc[t][nt][r] - ms.x * csv[nt][r]) * rs;
        *(u32x4*)(dst + t * 1024) = u32x4{pk(v[0], v[1]), pk(v[2], v[3]), pk(v[4], v[5]), pk(v[6], v[7])};
      }
    } else if (wave < 6) {
      char* dst = ws + OFF_V + ((size_t)h * 128 + T0 + 2 * mh) * KB + lane * 16;
      float2 ms[2][4];
#pragma unroll
      for (int t = 0; t < 2; ++t)
#pragma unroll
        for (int r = 0; r < 4; ++r) ms[t][r] = st[X * 64 + (2 * mh + t) * 16 + 4 * g + r];
#pragma unroll
      for (int nt = 0; nt < 2; ++nt) {
        float v[8];
#pragma unroll
        for (int t = 0; t < 2; ++t)
#pragma unroll
          for (int r = 0; r < 4; ++r) {
            float y = (acc[t][nt][r] - ms[t][r].x * csv[nt][0]) * ms[t][r].y;
            v[t * 4 + r] = (nt == 1 && i == 12) ? 1.f : y;
          }
        *(u32x4*)(dst + nt * 1024) = u32x4{pk(v[0], v[1]), pk(v[2], v[3]), pk(v[4], v[5]), pk(v[6], v[7])};
      }
    }
  }
}

template <int NP, bool FIRST>
__device__ __forceinline__ void attn_part(const char* ks, const char* vs, u32x4 q, int qt, int pl0, int plmin, int lane,
                                          float& m_out, f32x4 (&o)[2]) {
  const int i = lane & 15, g = lane >> 4;
  f32x4 s[NP][2];
  const float NEG = -__builtin_inff();
#pragma unroll
  for (int j = 0; j < NP; ++j) {
    const int pl = pl0 + j;
#pragma unroll
    for (int u = 0; u < 2; ++u) {
      if (pl >= plmin) {
        u32x4 kf = lds16(ks + (2 * pl + u) * 1024 + lane * 16);
        s[j][u] = mfma(kf, q, f32x4{0, 0, 0, 0});
        if ((FIRST && j == 0) || (!FIRST && j == NP - 1)) {
          int d0 = 16 * (qt + 16 - (2 * pl + u)) + i - 4 * g;
#pragma unroll
          for (int r = 0; r < 4; ++r) { int d = d0 - r; s[j][u][r] = (d >= 0 && d < 256) ? s[j][u][r] : NEG; }
        }
      } else {
        s[j][u] = f32x4{NEG, NEG, NEG, NEG};
      }
    }
  }
  float m = -1e30f;
#pragma unroll
  for (int j = 0; j < NP; ++j)
#pragma unroll
    for (int u = 0; u < 2; ++u)
#pragma unroll
      for (int r = 0; r < 4; ++r) m = fmaxf(m, s[j][u][r]);
  m = fmaxf(m, __shfl_xor(m, 16));
  m = fmaxf(m, __shfl_xor(m, 32));
  o[0] = f32x4{0, 0, 0, 0}; o[1] = f32x4{0, 0, 0, 0};
#pragma unroll
  for (int j = 0; j < NP; ++j) {
    const int pl = pl0 + j;
    if (pl >= plmin) {
      float pv[8];
#pragma unroll
      for (int u = 0; u < 2; ++u)
#pragma unroll
        for (int r = 0; r < 4; ++r) pv[u * 4 + r] = __builtin_amdgcn_exp2f(s[j][u][r] - m);
      u32x4 pf = {pk(pv[0], pv[1]), pk(pv[2], pv[3]), pk(pv[4], pv[5]), pk(pv[6], pv[7])};
#pragma unroll
      for (int dt = 0; dt < 2; ++dt) o[dt] = mfma(lds16(vs + (2 * pl + dt) * 1024 + lane * 16), pf, o[dt]);
    }
  }
  m_out = m;
}

__global__ __launch_bounds__(NT, 2) void k_attn(char* ws) {
  extern __shared__ __attribute__((aligned(16))) char sm[];
  int c, h; cmap(blockIdx.x, c, h);
  const int tid = threadIdx.x, lane = tid & 63, wave = __builtin_amdgcn_readfirstlane(tid >> 6);
  const int qt = wave & 3, kh = wave >> 2;
  u32x4 q[2];
  for (int X = 0; X < 2; ++X) {
    const int T0 = X * 64 + 4 * c;
    const int skip = T0 < 16 ? 16 - T0 : 0;
    dma(ws + OFF_K + ((size_t)h * 128 + T0 - 16 + skip) * KB, sm + (X * 40 + skip) * 1024, 20 - skip, wave, lane);
    dma(ws + OFF_V + ((size_t)h * 128 + T0 - 16 + skip) * KB, sm + (X * 40 + 20 + skip) * 1024, 20 - skip, wave, lane);
  }
  q[0] = ldg16(ws + OFF_Q + ((size_t)h * 128 + 4 * c + qt) * KB + lane * 16);
  q[1] = ldg16(ws + OFF_Q + ((size_t)h * 128 + 64 + 4 * c + qt) * KB + lane * 16);
  WAIT_VM0();
  __syncthreads();
  float* ex = (float*)(sm + 80 * 1024);
#pragma unroll
  for (int X = 0; X < 2; ++X) {
    const int T0 = X * 64 + 4 * c;
    const int plmin = T0 < 16 ? (16 - T0) >> 1 : 0;
    const char* ks = sm + X * 40 * 1024;
    const char* vs = ks + 20 * 1024;
    float m; f32x4 o[2];
    float* e = ex + (X * 4 + qt) * 9 * 64 + lane;
    if (kh == 0) {
      attn_part<5, true>(ks, vs, q[X], qt, qt >> 1, plmin, lane, m, o);
      e[0] = m;
#pragma unroll
      for (int k = 0; k < 8; ++k) e[(k + 1) * 64] = o[k >> 2][k & 3];
    } else {
      attn_part<4, false>(ks, vs, q[X], qt, (qt >> 1) + 5, plmin, lane, m, o);
    }
    __syncthreads();
    if (kh == 1) {
      float m0 = e[0], mt = fmaxf(m0, m);
      float a0 = __builtin_amdgcn_exp2f(m0 - mt), a1 = __builtin_amdgcn_exp2f(m - mt);
      float v[8];
#pragma unroll
      for (int k = 0; k < 8; ++k) v[k] = e[(k + 1) * 64] * a0 + o[k >> 2][k & 3] * a1;
      float l = __shfl(v[4], 48 + (lane & 15));
      float inv = __builtin_amdgcn_rcpf(l);
#pragma unroll
      for (int k = 0; k < 8; ++k) v[k] *= inv;
      *(u32x4*)(ws + OFF_O + ((size_t)(T0 + qt) * 16 + h) * KB + lane * 16) =
          u32x4{pk(v[0], v[1]), pk(v[2], v[3]), pk(v[4], v[5]), pk(v[6], v[7])};
    }
  }
}

__global__ __launch_bounds__(NT, 2) void k_oproj(char* ws, int xin, int xout) {
  extern __shared__ __attribute__((aligned(16))) char sm[];
  int c, m; cmap(blockIdx.x, c, m);
  const int tid = threadIdx.x, lane = tid & 63, wave = __builtin_amdgcn_readfirstlane(tid >> 6);
  const int i = lane & 15, g = lane >> 4;
  const int tt = m >> 2, J = 7 * (m & 3) + wave;
  for (int X = 0; X < 2; ++X) dma(ws + OFF_O + (size_t)(X * 64 + 4 * c + tt) * 16 * KB, sm + X * 16 * 1024, 16, wave, lane);
  u32x4 w[16];
  u32x2 xr[2];
  if (wave < 7) {
#pragma unroll
    for (int s = 0; s < 16; ++s) w[s] = ldg16(ws + OFF_WO + ((size_t)J * 16 + s) * KB + lane * 16);
#pragma unroll
    for (int X = 0; X < 2; ++X)
      xr[X] = *(const u32x2*)(ws + OFF_X + xin * X_BYTES + ((size_t)(X * 64 + 4 * c + tt) * 28 + J) * 512 + (i + 16 * (g & 1)) * 16 + 8 * (g >> 1));
  }
  WAIT_VM0();
  __syncthreads();
  if (wave < 7) {
#pragma unroll
    for (int X = 0; X < 2; ++X) {
      f32x4 a0 = {0, 0, 0, 0}, a1 = {0, 0, 0, 0};
#pragma unroll
      for (int s = 0; s < 16; s += 2) {
        a0 = mfma(w[s], lds16(sm + (X * 16 + s) * 1024 + lane * 16), a0);
        a1 = mfma(w[s + 1], lds16(sm + (X * 16 + s + 1) * 1024 + lane * 16), a1);
      }
      float v0 = a0[0] + a1[0] + bflo(xr[X].x), v1 = a0[1] + a1[1] + bfhi(xr[X].x);
      float v2 = a0[2] + a1[2] + bflo(xr[X].y), v3 = a0[3] + a1[3] + bfhi(xr[X].y);
      *(u32x2*)(ws + OFF_X + xout * X_BYTES + ((size_t)(X * 64 + 4 * c + tt) * 28 + J) * 512 + (i + 16 * (g & 1)) * 16 + 8 * (g >> 1)) =
          u32x2{pk(v0, v1), pk(v2, v3)};
    }
  }
}

__device__ __forceinline__ float geglu(float gt, float up) {
  float t = gt * gt;
  float z = gt * (t * (-2.f * 0.7978845608028654f * 0.044715f * 1.4426950408889634f) + (-2.f * 0.7978845608028654f * 1.4426950408889634f));
  float e = __builtin_amdgcn_exp2f(z);
  return gt * up * __builtin_amdgcn_rcpf(1.f + e);
}

__global__ __launch_bounds__(NT, 2) void k_gu(char* ws, int xin) {
  extern __shared__ __attribute__((aligned(16))) char sm[];
  int c, m; cmap(blockIdx.x, c, m);
  const int tid = threadIdx.x, lane = tid & 63, wave = __builtin_amdgcn_readfirstlane(tid >> 6);
  const int i = lane & 15, g = lane >> 4;
  const char* xf = ws + OFF_X + xin * X_BYTES;
  for (int X = 0; X < 2; ++X) dma(xf + (size_t)(X * 64 + 4 * c) * 14 * KB, sm + X * 56 * 1024, 56, wave, lane);
  float2* st = (float2*)(sm + 112 * 1024);
  const int J = 7 * m + wave;
  u32x4 w[14][2];
  f32x4 csg, csu;
  if (wave < 7) {
#pragma unroll
    for (int s = 0; s < 14; ++s)
#pragma unroll
      for (int u = 0; u < 2; ++u) w[s][u] = ldg16(ws + OFF_WGU + ((size_t)J * 28 + s * 2 + u) * KB + lane * 16);
    const float* cs = (const float*)(ws + OFF_CS) + 48 * 32;
    csg = *(const f32x4*)(cs + 16 * J + 4 * g);
    csu = *(const f32x4*)(cs + F_ + 16 * J + 4 * g);
  }
  WAIT_VM0();
  __syncthreads();
  for (int X = 0; X < 2; ++X) {
    const char* xs = sm + X * 56 * 1024;
    const int T0 = X * 64 + 4 * c;
    f32x4 ag[4] = {}, au[4] = {};
    if (wave < 7) {
#pragma unroll
      for (int s = 0; s < 14; ++s)
#pragma unroll
        for (int t = 0; t < 4; ++t) {
          u32x4 xv = lds16(xs + (t * 14 + s) * 1024 + lane * 16);
          ag[t] = mfma(w[s][0], xv, ag[t]);
          au[t] = mfma(w[s][1], xv, au[t]);
        }
    } else {
      ln_stats<4>(xs, st + X * 64, lane);
    }
    __syncthreads();
    if (wave < 7) {
      u32x2 hp[4];
#pragma unroll
      for (int t = 0; t < 4; ++t) {
        float2 ms = st[X * 64 + t * 16 + i];
        float hv[4];
#pragma unroll
        for (int r = 0; r < 4; ++r) hv[r] = geglu((ag[t][r] - ms.x * csg[r]) * ms.y, (au[t][r] - ms.x * csu[r]) * ms.y);
        hp[t] = u32x2{pk(hv[0], hv[1]), pk(hv[2], hv[3])};
      }
#pragma unroll
      for (int t = 0; t < 4; t += 2) {
        auto rx = __builtin_amdgcn_permlane32_swap(hp[t].x, hp[t + 1].x, false, false);
        auto ry = __builtin_amdgcn_permlane32_swap(hp[t].y, hp[t + 1].y, false, false);
        u32x4 o = {rx[0], ry[0], rx[1], ry[1]};
        *(u32x4*)(ws + OFF_H + ((size_t)(T0 + t + (g >> 1)) * 112 + J) * 512 + (lane & 31) * 16) = o;
      }
    }
  }
}

__global__ __launch_bounds__(NT, 2) void k_down(char* ws, int xin, int xout, uint16_t* out) {
  extern __shared__ __attribute__((aligned(16))) char sm[];
  int c, m; cmap(blockIdx.x, c, m);
  if (m >= 14) return;
  const int tid = threadIdx.x, lane = tid & 63, wave = __builtin_amdgcn_readfirstlane(tid >> 6);
  const int i = lane & 15, g = lane >> 4;
  const int th = m / 7, ng = m % 7;
  u32x4 w[7][4];
#pragma unroll
  for (int s = 0; s < 7; ++s)
#pragma unroll
    for (int nt = 0; nt < 4; ++nt) w[s][nt] = ldg16(ws + OFF_WD + (((size_t)ng * 56 + 7 * wave + s) * 4 + nt) * KB + lane * 16);
  const int ft = wave >> 2, J = 4 * ng + (wave & 3);
  for (int X = 0; X < 2; ++X) {
    const int T = X * 64 + 4 * c + 2 * th;
    u32x4 a[2][7];
#pragma unroll
    for (int t = 0; t < 2; ++t)
#pragma unroll
      for (int s = 0; s < 7; ++s) a[t][s] = ldg16(ws + OFF_H + ((size_t)(T + t) * 56 + 7 * wave + s) * KB + lane * 16);
    u32x2 xr = *(const u32x2*)(ws + OFF_X + xin * X_BYTES + ((size_t)(T + ft) * 28 + J) * 512 + (i + 16 * (g & 1)) * 16 + 8 * (g >> 1));
    f32x4 acc[2][4] = {};
#pragma unroll
    for (int s = 0; s < 7; ++s)
#pragma unroll
      for (int t = 0; t < 2; ++t)
#pragma unroll
        for (int nt = 0; nt < 4; ++nt) acc[t][nt] = mfma(w[s][nt], a[t][s], acc[t][nt]);
    char* red = sm + X * 64 * 1024;
#pragma unroll
    for (int t = 0; t < 2; ++t)
#pragma unroll
      for (int nt = 0; nt < 4; ++nt) *(f32x4*)(red + ((t * 4 + nt) * 8 + wave) * 1024 + lane * 16) = acc[t][nt];
    __syncthreads();
    f32x4 v = {bflo(xr.x), bfhi(xr.x), bflo(xr.y), bfhi(xr.y)};
#pragma unroll
    for (int k = 0; k < 8; ++k) v += *(const f32x4*)(red + (wave * 8 + k) * 1024 + lane * 16);
    u32x2 o = {pk(v[0], v[1]), pk(v[2], v[3])};
    if (out) *(u32x2*)(out + (size_t)((T + ft) * 16 + i) * D_ + 16 * J + 4 * g) = o;
    else *(u32x2*)(ws + OFF_X + xout * X_BYTES + ((size_t)(T + ft) * 28 + J) * 512 + (i + 16 * (g & 1)) * 16 + 8 * (g >> 1)) = o;
  }
}

extern "C" void kernel_launch(void* const* d_in, const int* in_sizes, int n_in, void* d_out, int out_size, void* d_ws,
                              size_t ws_size, hipStream_t stream) {
  const uint16_t* x = (const uint16_t*)d_in[0];
  const uint16_t* wqkv = (const uint16_t*)d_in[1];
  const uint16_t* wo = (const uint16_t*)d_in[2];
  const uint16_t* wg = (const uint16_t*)d_in[4];
  const uint16_t* wu = (const uint16_t*)d_in[5];
  const uint16_t* wd = (const uint16_t*)d_in[6];
  char* ws = (char*)d_ws;
  (void)hipFuncSetAttribute((const void*)k_qkv, hipFuncAttributeMaxDynamicSharedMemorySize, 114 * 1024);
  (void)hipFuncSetAttribute((const void*)k_attn, hipFuncAttributeMaxDynamicSharedMemorySize, 100 * 1024);
  (void)hipFuncSetAttribute((const void*)k_gu, hipFuncAttributeMaxDynamicSharedMemorySize, 114 * 1024);
  (void)hipFuncSetAttribute((const void*)k_down, hipFuncAttributeMaxDynamicSharedMemorySize, 128 * 1024);
  k_prep<<<256, NT, 0, stream>>>(x, wqkv, wo, wg, wu, wd, ws);
  for (int l = 0; l < 2; ++l) {
    k_qkv<<<256, NT, 114 * 1024, stream>>>(ws, 2 * l);
    k_attn<<<256, NT, 100 * 1024, stream>>>(ws);
    k_oproj<<<256, NT, 32 * 1024, stream>>>(ws, 2 * l, 2 * l + 1);
    k_gu<<<256, NT, 114 * 1024, stream>>>(ws, 2 * l + 1);
    k_down<<<256, NT, 128 * 1024, stream>>>(ws, 2 * l + 1, 2 * l + 2, l == 1 ? (uint16_t*)d_out : nullptr);
  }
}
```

```cpp
#include <hip/hip_runtime.h>
#include <hip/hip_bf16.h>
#include <stdint.h>
#include <type_traits>

typedef short bf16x8 __attribute__((ext_vector_type(8)));
typedef float f32x4 __attribute__((ext_vector_type(4)));
typedef float f32x2 __attribute__((ext_vector_type(2)));
typedef float f32x16 __attribute__((ext_vector_type(16)));
typedef unsigned u32x4 __attribute__((ext_vector_type(4)));
typedef unsigned u32x2 __attribute__((ext_vector_type(2)));
typedef __bf16 bf16x2_t __attribute__((ext_vector_type(2)));
#define GAS __attribute__((address_space(1)))
#define LAS __attribute__((address_space(3)))
typedef GAS unsigned gu32;
typedef volatile LAS unsigned* lflag;
#define RLX_AGENT __ATOMIC_RELAXED, __HIP_MEMORY_SCOPE_AGENT

constexpr int D_ = 448, DH = 28, F_ = 1792;
constexpr int NT = 512;
constexpr unsigned KB = 1024;

constexpr unsigned CNT_X2 = 0;
constexpr unsigned CNT_O  = CNT_X2 + 64 * 128;
constexpr unsigned CNT_X1 = CNT_O + 256 * 128;
constexpr unsigned CNT_H  = CNT_X1 + 128 * 128;
constexpr unsigned FLG_KV = CNT_H + 128 * 128;
constexpr unsigned CNT_IDLE = FLG_KV + 1024 * 128;
constexpr unsigned NCTRL = CNT_IDLE / 128;
constexpr unsigned OFF_WQKV = 256 * KB;
constexpr unsigned OFF_WO   = OFF_WQKV + 16 * 6 * 14 * KB;
constexpr unsigned OFF_WGU  = OFF_WO + 28 * 16 * KB;
constexpr unsigned OFF_WD   = OFF_WGU + 112 * 28 * KB;
constexpr unsigned OFF_CS   = OFF_WD + 7 * 56 * 4 * KB;
constexpr unsigned X_BYTES  = 128 * 28 * 512;
constexpr unsigned OFF_X    = OFF_CS + 32 * KB;
constexpr unsigned ST_BYTES = 128 * 28 * 128;
constexpr unsigned OFF_ST   = OFF_X + 4 * X_BYTES;
constexpr unsigned KV_BYTES = 16 * 64 * 4 * KB;
constexpr unsigned OFF_KV   = OFF_ST + 4 * ST_BYTES;
constexpr unsigned O_BYTES  = 128 * 16 * KB;
constexpr unsigned OFF_O    = OFF_KV + 2 * KV_BYTES;
constexpr unsigned H_BYTES  = 128 * 56 * KB;
constexpr unsigned OFF_H    = OFF_O + 2 * O_BYTES;
constexpr unsigned WS_USED  = OFF_H + 2 * H_BYTES;
constexpr unsigned OFF_STAMP = 64u << 20;

constexpr int L_REG = 69632;
constexpr int L_HALO = 24 * 1024, L_OWN = 56 * 1024, L_Q = 64 * 1024;
constexpr int L_RES = 2 * L_REG;
constexpr int L_ST = L_RES + 8192;
constexpr int L_CS = L_ST + 1024;
constexpr int L_FL = L_CS + 2048;
constexpr int L_TAB = L_FL + 256;
constexpr int L_STAMP = L_TAB + 1280;
constexpr int L_BYTES = L_STAMP + 8 * 128 * 4;
enum { F_RDY = 0, F_SRDY = 2, F_ARR1 = 4, F_ARR2 = 6, F_ARR3 = 8, F_ARR4 = 10, F_ARR5 = 12, F_RED = 14, F_ABORT = 16 };

__device__ __forceinline__ f32x4 mfma(u32x4 a, u32x4 b, f32x4 c) {
  return __builtin_amdgcn_mfma_f32_16x16x32_bf16(__builtin_bit_cast(bf16x8, a), __builtin_bit_cast(bf16x8, b), c, 0, 0, 0);
}
__device__ __forceinline__ f32x16 mfma32(u32x4 a, u32x4 b, f32x16 c) {
  return __builtin_amdgcn_mfma_f32_32x32x16_bf16(__builtin_bit_cast(bf16x8, a), __builtin_bit_cast(bf16x8, b), c, 0, 0, 0);
}
__device__ __forceinline__ unsigned pk(float a, float b) {
  bf16x2_t v = {(__bf16)a, (__bf16)b};
  return __builtin_bit_cast(unsigned, v);
}
__device__ __forceinline__ unsigned pks(float a, float b) {
  unsigned r;
  asm("v_cvt_pk_bf16_f32 %0, %1, %2" : "=v"(r) : "v"(a), "v"(b));
  return r;
}
__device__ __forceinline__ float bflo(unsigned u) { return __builtin_bit_cast(float, u << 16); }
__device__ __forceinline__ float bfhi(unsigned u) { return __builtin_bit_cast(float, u & 0xffff0000u); }
__device__ __forceinline__ unsigned f2u(float f) { return __builtin_bit_cast(unsigned, f); }
__device__ __forceinline__ float u2f(unsigned u) { return __builtin_bit_cast(float, u); }
__device__ __forceinline__ u32x4 lds16(const char* p) { return *(const u32x4*)p; }
#define FW(a, b) (a)
#define SCHED() __builtin_amdgcn_sched_barrier(0)
#define OPQ(x) asm("" : "+v"(x))
#define PRIO(n) __builtin_amdgcn_s_setprio(n)
#define WAIT_VM(n) asm volatile("s_waitcnt vmcnt(" #n ")" ::: "memory")
#define WAIT_ALL() asm volatile("s_waitcnt vmcnt(0) lgkmcnt(0)" ::: "memory")
#define WAIT_LGKM() asm volatile("s_waitcnt lgkmcnt(0)" ::: "memory")
typedef __amdgpu_buffer_rsrc_t rsrc_t;
__device__ __forceinline__ u32x4 ldw(rsrc_t rs, unsigned voff, unsigned soff) { return __builtin_amdgcn_raw_buffer_load_b128(rs, voff, soff, 0); }
__device__ __forceinline__ u32x4 ldh(rsrc_t rs, unsigned voff, unsigned soff) { return __builtin_amdgcn_raw_buffer_load_b128(rs, voff, soff, 16); }
__device__ __forceinline__ u32x4 ldwf(rsrc_t rs, unsigned l16, unsigned base, int k) { return ldw(rs, l16 + (k & 3) * 1024, base + (k >> 2) * 4096); }
#define WCNT(n) #n
#define WAIT_W(n) asm volatile("s_waitcnt vmcnt(" WCNT(n) ") lgkmcnt(0)" ::: "memory")
__device__ __forceinline__ u32x4 ldhf(rsrc_t rs, unsigned l16, unsigned base, int k) { return ldh(rs, l16 + (k & 3) * 1024, base + (k >> 2) * 4096); }
__device__ __forceinline__ unsigned opaque(unsigned v) { asm volatile("" : "+s"(v)); return v; }
template <int N> __device__ __forceinline__ void touch(const u32x4* w) {
#pragma unroll
  for (int k = 0; k < N; ++k) asm volatile("" :: "v"(w[k]));
}
__device__ __forceinline__ int opaque_v(int v) { asm volatile("" : "+v"(v)); return v; }
__device__ __forceinline__ void sth(u32x4 v, char* ws, unsigned voff, unsigned soff) {
  asm volatile("global_store_dwordx4 %0, %1, %2 sc1\n\ts_nop 1" :: "v"(voff + soff), "v"(v), "s"(ws) : "memory");
}
__device__ __forceinline__ void sth32(unsigned v, char* ws, unsigned voff, unsigned soff) {
  asm volatile("global_store_dword %0, %1, %2 sc1" :: "v"(voff + soff), "v"(v), "s"(ws) : "memory");
}

#define STAMP() do {} while (0)

__device__ __forceinline__ int kk_perm1(int g, int e) { return 4 * g + (e & 3) + 16 * (e >> 2); }
__device__ __forceinline__ int kk_perm2(int g, int e) { return 16 * (g >> 1) + 4 * (g & 1) + (e & 3) + 8 * (e >> 2); }

__global__ __launch_bounds__(NT) void k_prep(const uint16_t* __restrict__ x, const uint16_t* __restrict__ wqkv,
                                             const uint16_t* __restrict__ wo, const uint16_t* __restrict__ wg,
                                             const uint16_t* __restrict__ wu, const uint16_t* __restrict__ wd, char* ws) {
  __shared__ __attribute__((aligned(16))) char sm[448 * 72 + 8 * 32 * 4];
  const int b = blockIdx.x, tid = threadIdx.x, lane = tid & 63, wave = tid >> 6;
  if (tid < 7 && b * 7 + tid < (int)NCTRL) *(unsigned*)(ws + (b * 7 + tid) * 128) = 0u;
  if (b >= 230) {
    char* xf = ws + OFF_X;
    float* st = (float*)(ws + OFF_ST);
    u32x2 a[9], c[9];
#pragma unroll
    for (int k = 0; k < 9; ++k) {
      int ch = min((b - 230) * NT + tid + k * 26 * NT, 128 * 896 - 1);
      int tile = ch / 896, r = ch % 896, J = r >> 5, lp = r & 31, i = lp & 15, gp = lp >> 4;
      const uint16_t* src = x + (size_t)(tile * 16 + i) * D_ + 16 * J + 4 * gp;
      a[k] = *(const u32x2*)src; c[k] = *(const u32x2*)(src + 8);
    }
#pragma unroll
    for (int k = 0; k < 9; ++k) {
      int ch = (b - 230) * NT + tid + k * 26 * NT;
      float v[8] = {bflo(a[k].x), bfhi(a[k].x), bflo(a[k].y), bfhi(a[k].y), bflo(c[k].x), bfhi(c[k].x), bflo(c[k].y), bfhi(c[k].y)};
      float s1 = 0.f, s2 = 0.f;
#pragma unroll
      for (int e = 0; e < 8; ++e) { s1 += v[e]; s2 += v[e] * v[e]; }
      s1 += __shfl_xor(s1, 16); s2 += __shfl_xor(s2, 16);
      if (ch < 128 * 896) {
        const int tile = ch / 896, r = ch % 896;
        *(u32x4*)(xf + (size_t)((tile >> 1) * 28 + (r >> 5)) * 1024 + (tile & 1) * 512 + (r & 31) * 16) = u32x4{a[k].x, a[k].y, c[k].x, c[k].y};
        st[ch] = (ch & 16) ? s2 : s1;
      }
    }
    return;
  }
  const uint16_t* src; int ld, ncols = 32, type; char* dst; int csf = -1, csb = 0; int s_stride, nt_stride;
  if (b < 48) {
    int h = b / 3, p = b % 3; type = 0;
    src = wqkv + p * D_ + DH * h; ld = 3 * D_; ncols = DH;
    dst = ws + OFF_WQKV + (size_t)(h * 3 + p) * 28 * KB; s_stride = 1024; nt_stride = 14 * 1024;
    csf = h * 112 + p * 32;
  } else if (b < 62) {
    int j = b - 48; type = 1;
    src = wo + 32 * j; ld = D_;
    dst = ws + OFF_WO + (size_t)(2 * j) * 16 * KB; s_stride = 1024; nt_stride = 16 * 1024;
  } else if (b < 174) {
    int j = (b - 62) >> 1, u = (b - 62) & 1; type = 2;
    src = (u ? wu : wg) + 32 * j; ld = F_;
    dst = ws + OFF_WGU + (size_t)(2 * j) * 28 * KB + u * 256; s_stride = 1024; nt_stride = 28 * 1024;
    csf = 32 * j; csb = 96 + 112 * u;
  } else {
    int j = (b - 174) >> 2, q = (b - 174) & 3; type = 2;
    src = wd + (size_t)q * 448 * D_ + 32 * j; ld = D_;
    dst = ws + OFF_WD + ((size_t)(j >> 1) * 112 + q * 28) * 2 * KB + (j & 1) * KB; s_stride = 2048; nt_stride = 256;
  }
  {
    u32x2 v[7];
    const int c4 = tid & 7;
    const bool ok = c4 * 4 < ncols;
#pragma unroll
    for (int u = 0; u < 7; ++u) v[u] = *(const u32x2*)(src + (size_t)(u * 64 + (tid >> 3)) * ld + (ok ? c4 * 4 : 0));
#pragma unroll
    for (int u = 0; u < 7; ++u) *(u32x2*)(sm + (u * 64 + (tid >> 3)) * 72 + c4 * 8) = ok ? v[u] : u32x2{0u, 0u};
  }
  __syncthreads();
  const int i = lane & 15, g = lane >> 4;
  const int nfr = type == 1 ? 32 : 28;
  for (int f = wave; f < nfr; f += 8) {
    const int s = type == 2 ? f : f >> 1, nt = type == 2 ? (g & 1) : (f & 1);
    unsigned e16[8];
#pragma unroll
    for (int e = 0; e < 8; ++e) {
      int row; bool ok = true;
      if (type == 1) { int kk = kk_perm1(g, e); ok = kk < DH; row = DH * s + (ok ? kk : 0); }
      else if (type == 0) row = 32 * s + kk_perm2(g, e);
      else row = 16 * s + 4 * (g >> 1) + (e & 3) + 8 * (e >> 2);
      unsigned v = *(const uint16_t*)(sm + row * 72 + (16 * nt + i) * 2);
      e16[e] = ok ? v : 0u;
    }
    u32x4 o = {e16[0] | (e16[1] << 16), e16[2] | (e16[3] << 16), e16[4] | (e16[5] << 16), e16[6] | (e16[7] << 16)};
    const int lo = type == 2 ? i * 16 + (g >> 1) * 512 : lane * 16;
    *(u32x4*)(dst + (size_t)s * s_stride + (size_t)nt * nt_stride + lo) = o;
  }
  if (csf >= 0) {
    int col = tid & 31, rg = tid >> 5;
    float a = 0.f;
#pragma unroll
    for (int r = 0; r < 28; ++r) a += bflo(*(const uint16_t*)(sm + (rg * 28 + r) * 72 + col * 2));
    a += __shfl_xor(a, 32);
    float* red = (float*)(sm + 448 * 72);
    if (lane < 32) red[wave * 32 + col] = a;
    __syncthreads();
    if (tid < 32) {
      float t = 0.f;
#pragma unroll
      for (int w = 0; w < 8; ++w) t += red[w * 32 + tid];
      const int f = csf + tid;
      ((float*)(ws + OFF_CS))[(f / 112) * 512 + csb + f % 112] = t;
    }
  }
}

__device__ __forceinline__ void lwait(lflag fl, int idx, unsigned tgt) {
  unsigned v, sv, n = 1u << 16;
  const unsigned addr = (unsigned)(uintptr_t)(fl + idx);
  asm volatile("1:\n\tds_read_b32 %0, %3\n\ts_waitcnt lgkmcnt(0)\n\tv_readfirstlane_b32 %1, %0\n\ts_cmp_ge_u32 %1, %4\n\t"
               "s_cbranch_scc1 2f\n\ts_sleep 1\n\ts_sub_u32 %2, %2, 1\n\ts_cbranch_scc0 1b\n2:"
               : "=&v"(v), "=&s"(sv), "+s"(n) : "v"(addr), "s"(tgt) : "memory", "scc");
}
__device__ __forceinline__ bool lready(lflag fl, int idx, unsigned tgt) {
  bool r = (unsigned)__builtin_amdgcn_readfirstlane(fl[idx]) >= tgt;
  asm volatile("" ::: "memory");
  return r;
}
__device__ __forceinline__ bool arrive(lflag fl, int idx, unsigned tgt) {
  unsigned old, so;
  const unsigned addr = (unsigned)(uintptr_t)(fl + idx), one = 1u;
  asm volatile("s_mov_b64 exec, 1\n\tds_add_rtn_u32 %0, %2, %3\n\ts_mov_b64 exec, -1\n\ts_waitcnt lgkmcnt(0)\n\tv_readfirstlane_b32 %1, %0"
               : "=&v"(old), "=s"(so) : "v"(addr), "v"(one) : "memory");
  return so + 1u == tgt;
}
__device__ __forceinline__ void arrive_nr(lflag fl, int idx) {
  const unsigned addr = (unsigned)(uintptr_t)(fl + idx), one = 1u;
  asm volatile("s_mov_b64 exec, 1\n\tds_add_u32 %0, %1\n\ts_mov_b64 exec, -1" :: "v"(addr), "v"(one) : "memory");
}
__device__ __forceinline__ void gadd(char* ws, unsigned off) {
  const unsigned one = 1u;
  asm volatile("s_mov_b64 exec, 1\n\tglobal_atomic_add %0, %1, %2\n\ts_mov_b64 exec, -1" :: "v"(off), "v"(one), "s"(ws) : "memory");
}
__device__ __forceinline__ void dma8k(const char* ws, unsigned voff, unsigned lds) {
  unsigned keep;
  lds = __builtin_amdgcn_readfirstlane(lds + 4096);
  asm volatile("s_mov_b32 %0, m0\n\ts_mov_b32 m0, %3\n\ts_nop 0\n\t"
               "global_load_lds_dwordx4 %1, %2 offset:-4096 sc1\n\tglobal_load_lds_dwordx4 %1, %2 offset:-3072 sc1\n\t"
               "global_load_lds_dwordx4 %1, %2 offset:-2048 sc1\n\tglobal_load_lds_dwordx4 %1, %2 offset:-1024 sc1\n\t"
               "global_load_lds_dwordx4 %1, %2 sc1\n\tglobal_load_lds_dwordx4 %1, %2 offset:1024 sc1\n\t"
               "global_load_lds_dwordx4 %1, %2 offset:2048 sc1\n\tglobal_load_lds_dwordx4 %1, %2 offset:3072 sc1\n\t"
               "s_mov_b32 m0, %0"
               : "=&s"(keep) : "v"(voff + 4096), "s"(ws), "s"(lds) : "memory");
}
__device__ __forceinline__ void dma4k(const char* ws, unsigned voff, unsigned lds) {
  unsigned keep;
  lds = __builtin_amdgcn_readfirstlane(lds);
  asm volatile("s_mov_b32 %0, m0\n\ts_mov_b32 m0, %3\n\ts_nop 0\n\t"
               "global_load_lds_dwordx4 %1, %2 sc1\n\tglobal_load_lds_dwordx4 %1, %2 offset:1024 sc1\n\t"
               "global_load_lds_dwordx4 %1, %2 offset:2048 sc1\n\tglobal_load_lds_dwordx4 %1, %2 offset:3072 sc1\n\t"
               "s_mov_b32 m0, %0"
               : "=&s"(keep) : "v"(voff), "s"(ws), "s"(lds) : "memory");
}
__device__ __forceinline__ void dma1k(const char* ws, unsigned voff, unsigned lds) {
  unsigned keep;
  lds = __builtin_amdgcn_readfirstlane(lds);
  asm volatile("s_mov_b32 %0, m0\n\ts_mov_b32 m0, %3\n\ts_nop 0\n\tglobal_load_lds_dwordx4 %1, %2 sc1\n\ts_mov_b32 m0, %0"
               : "=&s"(keep) : "v"(voff), "s"(ws), "s"(lds) : "memory");
}

constexpr float GC1 = -2.f * 0.7978845608028654f * 1.4426950408889634f, GC2 = GC1 * 0.044715f;
__device__ __forceinline__ u32x4 pair16(u32x2 a, u32x2 b) {
  auto rx = __builtin_amdgcn_permlane32_swap(a.x, b.x, false, false);
  auto ry = __builtin_amdgcn_permlane32_swap(a.y, b.y, false, false);
  return u32x4{rx[0], ry[0], rx[1], ry[1]};
}
__device__ __forceinline__ float allg_max(float m) {
  auto r = __builtin_amdgcn_permlane32_swap(f2u(m), f2u(m), false, false);
  m = fmaxf(u2f(r[0]), u2f(r[1]));
  auto q = __builtin_amdgcn_permlane16_swap(f2u(m), f2u(m), false, false);
  return fmaxf(u2f(q[0]), u2f(q[1]));
}

__device__ __forceinline__ void serve_x(char* ws, char* sm, unsigned smb, lflag fl, int xi, int T0, int X, unsigned stage, int lane) {
  const unsigned src = OFF_X + xi * X_BYTES + (unsigned)T0 * 14 * KB + lane * 16;
#pragma unroll
  for (int k = 0; k < 7; ++k) dma8k(ws, src + k * 8192, smb + X * L_REG + k * 8192);
  u32x4 p[14];
  const unsigned vo = (lane >> 4) * 3584 + (lane & 15) * 16 + OFF_ST + xi * ST_BYTES + (unsigned)T0 * 3584;
  asm volatile("global_load_dwordx4 %0, %14, %15 sc1\n\tglobal_load_dwordx4 %1, %14, %15 offset:256 sc1\n\t"
               "global_load_dwordx4 %2, %14, %15 offset:512 sc1\n\tglobal_load_dwordx4 %3, %14, %15 offset:768 sc1\n\t"
               "global_load_dwordx4 %4, %14, %15 offset:1024 sc1\n\tglobal_load_dwordx4 %5, %14, %15 offset:1280 sc1\n\t"
               "global_load_dwordx4 %6, %14, %15 offset:1536 sc1\n\tglobal_load_dwordx4 %7, %14, %15 offset:1792 sc1\n\t"
               "global_load_dwordx4 %8, %14, %15 offset:2048 sc1\n\tglobal_load_dwordx4 %9, %14, %15 offset:2304 sc1\n\t"
               "global_load_dwordx4 %10, %14, %15 offset:2560 sc1\n\tglobal_load_dwordx4 %11, %14, %15 offset:2816 sc1\n\t"
               "global_load_dwordx4 %12, %14, %15 offset:3072 sc1\n\tglobal_load_dwordx4 %13, %14, %15 offset:3328 sc1\n\t"
               "s_waitcnt vmcnt(14)"
               : "=&v"(p[0]), "=&v"(p[1]), "=&v"(p[2]), "=&v"(p[3]), "=&v"(p[4]), "=&v"(p[5]), "=&v"(p[6]), "=&v"(p[7]), "=&v"(p[8]),
                 "=&v"(p[9]), "=&v"(p[10]), "=&v"(p[11]), "=&v"(p[12]), "=&v"(p[13])
               : "v"(vo), "s"(ws) : "memory");
  fl[F_RDY + X] = stage;
  asm volatile("s_waitcnt vmcnt(0)"
               : "+v"(p[0]), "+v"(p[1]), "+v"(p[2]), "+v"(p[3]), "+v"(p[4]), "+v"(p[5]), "+v"(p[6]), "+v"(p[7]), "+v"(p[8]), "+v"(p[9]),
                 "+v"(p[10]), "+v"(p[11]), "+v"(p[12]), "+v"(p[13]) :: "memory");
  float a[4];
#pragma unroll
  for (int r = 0; r < 4; ++r) {
    float t0 = u2f(p[0][r]) + u2f(p[1][r]), t1 = u2f(p[2][r]) + u2f(p[3][r]);
    OPQ(t0); OPQ(t1);
#pragma unroll
    for (int k = 4; k < 14; k += 2) { t0 += u2f(p[k][r]); t1 += u2f(p[k + 1][r]); OPQ(t0); OPQ(t1); }
    a[r] = t0 + t1;
  }
  float ms[4], rsd[4];
#pragma unroll
  for (int r = 0; r < 4; ++r) {
    float s1 = a[r] + u2f(__builtin_amdgcn_update_dpp(0u, f2u(a[r]), 0x128, 0xf, 0xf, false));
    float s2 = u2f(__builtin_amdgcn_update_dpp(0u, f2u(s1), 0x124, 0xf, 0xf, false));
    float mean = s1 * (1.f / D_);
    OPQ(mean);
    float var = fmaxf(__builtin_fmaf(-mean, mean, s2 * (1.f / D_)), 0.f);
    ms[r] = mean; rsd[r] = __builtin_amdgcn_rsqf(var + 1e-5f);
  }
  if ((lane & 15) < 4) {
    char* d = sm + L_ST + X * 512 + ((lane >> 4) * 16 + (lane & 3) * 4) * 8;
    *(f32x4*)d = f32x4{ms[0], rsd[0], ms[1], rsd[1]};
    *(f32x4*)(d + 16) = f32x4{ms[2], rsd[2], ms[3], rsd[3]};
  }
  asm volatile("" ::: "memory");
  fl[F_SRDY + X] = stage;
}

template <bool EDGE>
__device__ __forceinline__ u32x4 attn_tile(const char* kv, u32x4 q, int qt, int nskip, int lane) {
  const int i = lane & 15, g = lane >> 4;
  const float NEG = -__builtin_inff();
  f32x4 s[18];
  u32x4 kf[18];
#pragma unroll
  for (int j = 0; j < 6; ++j) kf[j] = lds16(kv + (j >> 1) * 4096 + (j & 1) * 1024);
#pragma unroll
  for (int j = 0; j < 18; ++j) {
    if (j + 6 < 18) kf[j + 6] = lds16(kv + ((j + 6) >> 1) * 4096 + (j & 1) * 1024);
    SCHED();
    s[j] = mfma(kf[j], q, f32x4{0.f, 0.f, 0.f, 0.f});
    SCHED();
  }
  u32x4 vf[18];
#pragma unroll
  for (int j = 0; j < 4; ++j) vf[j] = lds16(kv + (j >> 1) * 4096 + 2048 + (j & 1) * 1024);
  const int dq = 16 * (qt & 1) + i - 4 * g;
#pragma unroll
  for (int u = 0; u < 2; ++u)
#pragma unroll
    for (int r = 0; r < 4; ++r) {
      const bool lo = dq < 16 * u + r;
      s[u][r] = lo ? s[u][r] : NEG;
      s[16 + u][r] = lo ? NEG : s[16 + u][r];
    }
  if (EDGE) {
#pragma unroll
    for (int j = 0; j < 16; ++j)
#pragma unroll
      for (int r = 0; r < 4; ++r) s[j][r] = (j >> 1) < nskip ? NEG : s[j][r];
  }
  float mx[4];
#pragma unroll
  for (int r = 0; r < 4; ++r) mx[r] = fmaxf(s[2][r], s[3][r]);
#pragma unroll
  for (int j = 4; j < 20; j += 2)
#pragma unroll
    for (int r = 0; r < 4; ++r) mx[r] = fmaxf(fmaxf(mx[r], s[j % 18][r]), s[(j + 1) % 18][r]);
  const float m = allg_max(fmaxf(fmaxf(mx[0], mx[1]), fmaxf(mx[2], mx[3])));
  const f32x4 m4 = {m, m, m, m};
  SCHED();
#pragma unroll
  for (int j = 0; j < 18; ++j) s[j] -= m4;
  SCHED();
  f32x4 o0 = {0.f, 0.f, 0.f, 0.f}, o1 = {0.f, 0.f, 0.f, 0.f};
#pragma unroll
  for (int j = 0; j < 9; ++j) {
    if (j + 2 < 9) { vf[2 * j + 4] = lds16(kv + (j + 2) * 4096 + 2048); vf[2 * j + 5] = lds16(kv + (j + 2) * 4096 + 3072); }
    float p[8];
#pragma unroll
    for (int k = 0; k < 8; ++k) p[k] = __builtin_amdgcn_exp2f(s[2 * j + (k >> 2)][k & 3]);
    const u32x4 pf = {pk(p[0], p[1]), pk(p[2], p[3]), pk(p[4], p[5]), pk(p[6], p[7])};
    if (!EDGE || j >= nskip) {
      o0 = mfma(vf[2 * j], pf, o0);
      o1 = mfma(vf[2 * j + 1], pf, o1);
    }
    SCHED();
  }
  auto r = __builtin_amdgcn_permlane32_swap(f2u(o1[0]), f2u(o1[0]), false, false);
  auto t = __builtin_amdgcn_permlane16_swap(r[1], r[1], false, false);
  const float inv = __builtin_amdgcn_rcpf(u2f(t[1]));
  return u32x4{pk(o0[0] * inv, o0[1] * inv), pk(o0[2] * inv, o0[3] * inv), pk(o1[0] * inv, o1[1] * inv), pk(o1[2] * inv, o1[3] * inv)};
}

__device__ __forceinline__ u32x4 geglu8(const f32x16& acc, f32x2 ms, const f32x4& cg0, const f32x4& cg1, const f32x4& cu0, const f32x4& cu1) {
  const float q = ms.y * ms.y, k1 = GC1 * ms.y, k2 = GC2 * ms.y * q, ir = __builtin_amdgcn_rcpf(q), nm = -ms.x;
  float a[8], b[8], t[8];
#pragma unroll
  for (int r = 0; r < 8; ++r) { a[r] = __builtin_fmaf(nm, r < 4 ? cg0[r & 3] : cg1[r & 3], acc[r]); OPQ(a[r]); }
  SCHED();
#pragma unroll
  for (int r = 0; r < 8; ++r) { t[r] = a[r] * a[r]; OPQ(t[r]); }
  SCHED();
#pragma unroll
  for (int r = 0; r < 8; ++r) { t[r] = __builtin_fmaf(t[r], k2, k1); OPQ(t[r]); }
  SCHED();
#pragma unroll
  for (int r = 0; r < 8; ++r) { t[r] = t[r] * a[r]; OPQ(t[r]); }
  SCHED();
#pragma unroll
  for (int r = 0; r < 8; ++r) {
    t[r] = __builtin_amdgcn_exp2f(t[r]);
    b[r] = __builtin_fmaf(nm, r < 4 ? cu0[r & 3] : cu1[r & 3], acc[r + 8]); OPQ(b[r]);
  }
  SCHED();
#pragma unroll
  for (int r = 0; r < 8; ++r) { t[r] = __builtin_fmaf(t[r], ir, ir); OPQ(t[r]); }
  SCHED();
#pragma unroll
  for (int r = 0; r < 8; ++r) { t[r] = __builtin_amdgcn_rcpf(t[r]); b[r] = a[r] * b[r]; OPQ(b[r]); }
  SCHED();
#pragma unroll
  for (int r = 0; r < 8; ++r) { t[r] = t[r] * b[r]; OPQ(t[r]); }
  SCHED();
  return u32x4{pks(t[0], t[1]), pks(t[2], t[3]), pks(t[4], t[5]), pks(t[6], t[7])};
}

#define IDS()                                                                                                   \
  const int lane = opaque_v(lane0), wave = opaque(wave0), b = opaque(b0), xcd = b & 7, rk = b >> 3;             \
  const int c1 = 4 * (xcd >> 1) + (rk & 3), h1 = 8 * (xcd & 1) + (rk >> 2), c3 = 2 * xcd + (rk & 1), m3 = rk >> 1; \
  const bool act5 = m3 < 14;                                                                                    \
  const int th = m3 >= 7 ? 1 : 0, ng = m3 - 7 * th, i = lane & 15, g = lane >> 4;                               \
  const int part = wave < 6 ? 2 - (wave >> 1) : 0, nt1 = wave & 1, nt6 = 2 * part + nt1, J4 = 7 * h1 + wave, J3 = 7 * (m3 & 3) + wave; \
  const unsigned l16 = lane * 16, half_off = (lane & 31) * 16, res_off = (i + 16 * (g & 1)) * 16 + 8 * (g >> 1), f32_off = (g & 1) * 512 + (i + 16 * (g >> 1)) * 16; \
  (void)c1; (void)h1; (void)c3; (void)act5; (void)ng; (void)part; (void)nt1; (void)J4; (void)J3; (void)l16; (void)half_off; (void)res_off; (void)f32_off; (void)nt6
__global__ __launch_bounds__(NT, 2) void k_main(char* ws, uint16_t* out) {
  extern __shared__ __attribute__((aligned(16))) char sm[];
  const int tid = threadIdx.x, lane0 = tid & 63, wave0 = __builtin_amdgcn_readfirstlane(tid >> 6), b0 = blockIdx.x;
  IDS();
  lflag fl = (lflag)(LAS char*)(sm + L_FL);
  const rsrc_t rs = __builtin_amdgcn_make_buffer_rsrc(ws, 0, WS_USED, 0x00020000);
  int stn = 0; (void)stn;
  if (tid < 64) fl[tid] = 0u;
  __syncthreads();

  if (wave == 7) {
    const unsigned smb = (unsigned)(uintptr_t)(LAS char*)sm;
    PRIO(3);
    dma1k(ws, OFF_CS + h1 * 2048 + l16, smb + L_CS);
    dma1k(ws, OFF_CS + h1 * 2048 + 1024 + l16, smb + L_CS + 1024);
    int sgA = 0, sgB = 0;
    unsigned pubA = 0, pubB = 0;
    u32x4 dep = {CNT_IDLE, 0u, (unsigned)L_FL, 0u};
    bool rA = true, rB = false;
#pragma unroll 1
    for (int it = 0; it < (1 << 16); ++it) {
      if (it) {
        const unsigned lv = *(volatile LAS unsigned*)(LAS char*)(sm + dep.z);
        const unsigned long long lok = __ballot(lv >= dep.w);
        const bool mine = (lane >> 5) ? (unsigned)(lok >> 32) == 0xffffffffu : (unsigned)lok == 0xffffffffu;
        unsigned gv;
        asm volatile("global_load_dword %0, %1, %2 sc1\n\ts_waitcnt vmcnt(1)" : "=v"(gv) : "v"(mine ? dep.x : CNT_IDLE + b * 128), "s"(ws) : "memory");
        if (pubA) { fl[pubA >> 8] = pubA & 255u; pubA = 0; STAMP(); }
        if (pubB) { fl[pubB >> 8] = pubB & 255u; pubB = 0; STAMP(); }
        asm volatile("s_waitcnt vmcnt(0)" : "+v"(gv) :: "memory");
        if (sgA >= 10 && sgB >= 10) break;
        const unsigned long long ok = __ballot(mine && gv >= dep.y);
        rA = sgA < 10 && (unsigned)ok == 0xffffffffu; rB = sgB < 10 && (unsigned)(ok >> 32) == 0xffffffffu;
        if (!rA && !rB) continue;
        asm volatile("" ::: "memory");
      }
      const int X = rA ? 0 : 1, sg = rA ? sgA : sgB, l = sg >= 5 ? 1 : 0, p = sg - 5 * l;
      unsigned pub = ((F_RDY + X) << 8) | (sg + 1);
      STAMP();
      if (p == 0 || p == 3) {
        serve_x(ws, sm, smb, fl, 2 * l + (p == 3 ? 1 : 0), X * 64 + 4 * c1, X, sg + 1, lane);
        pub = 0;
      } else if (p == 1) {
        const int gi = X * 16 + c1, nsk = gi < 4 ? 4 - gi : 0;
        const unsigned src = OFF_KV + l * KV_BYTES + (h1 * 64 + 2 * gi - 8) * 4096 + l16;
#pragma unroll
        for (int k = 0; k < 4; ++k) if (k >= nsk) dma8k(ws, src + k * 8192, smb + X * L_REG + L_HALO + k * 8192);
      } else if (p == 2) {
        const int T = X * 64 + 4 * c3 + (m3 >> 2);
        const unsigned src = OFF_O + l * O_BYTES + T * 16 * KB + l16;
        dma8k(ws, src, smb + X * L_REG);
        dma8k(ws, src + 8192, smb + X * L_REG + 8192);
        const unsigned rsrc = OFF_X + 2 * l * X_BYTES + ((T >> 1) * 28 + 7 * (m3 & 3) + (lane >> 5)) * KB + (T & 1) * 512 + half_off;
#pragma unroll
        for (int k = 0; k < 4; ++k) dma1k(ws, rsrc + k * 2048, smb + L_RES + X * 4096 + k * 1024);
      } else if (act5) {
        fl[F_RDY + X] = sg + 1;
        const int P = X * 32 + 2 * c3 + th;
        const unsigned src = OFF_H + l * H_BYTES + P * 112 * KB + f32_off;
#pragma unroll
        for (int k = 0; k < 7; ++k) dma8k(ws, src + k * 16 * KB, smb + X * L_REG + k * 8192);
        dma4k(ws, OFF_X + (2 * l + 1) * X_BYTES + (P * 28 + 4 * ng) * KB + f32_off, smb + L_RES + X * 4096);
        pub = ((F_SRDY + X) << 8) | (sg + 1);
      }
      if (rA) { pubA = pub; ++sgA; } else { pubB = pub; ++sgB; }
      if (it == 0) {
        for (int e = lane; e < 80; e += 64) {
          const int sg = e >> 3, X = (e >> 2) & 1, sub = e & 3, l = sg >= 5 ? 1 : 0, p = sg - 5 * l, gi = X * 16 + c1;
          unsigned go, gt, li = F_ARR1 + X, lt = 0;
          if (p == 0) { go = CNT_X2 + (X * 32 + 2 * c1 + (sub & 1)) * 128; gt = 7 * l; li = (act5 ? F_ARR5 : F_ARR4) + X; lt = (act5 ? 4 : 7) * l; }
          else if (p == 1) { go = FLG_KV + ((l * 16 + h1) * 32 + max(gi - 4 + sub, 0)) * 128; gt = gi > 0 ? 4 : 0; lt = 6 * (l + 1); }
          else if (p == 2) { go = CNT_O + (l * 128 + X * 64 + 4 * c3 + (m3 >> 2)) * 128; gt = 16; li = F_ARR2 + X; lt = 4 * (l + 1); }
          else if (p == 3) { go = CNT_X1 + (l * 64 + X * 32 + 2 * c1 + (sub & 1)) * 128; gt = 8; li = ((sub & 1) ? F_ARR3 : F_ARR2) + X; lt = ((sub & 1) ? 7 : 4) * (l + 1); }
          else { go = CNT_H + (l * 32 + X * 16 + c3) * 128; gt = act5 ? 16 : 0; li = F_ARR4 + X; lt = 7 * (l + 1); }
          *(u32x4*)(sm + L_TAB + e * 16) = u32x4{go, gt, L_FL + li * 4, lt};
        }
      }
      dep = lds16(sm + L_TAB + (min((lane >> 5) ? sgB : sgA, 9) * 8 + (lane >> 5) * 4 + (lane & 3)) * 16);
      STAMP();
    }
  } else {
    u32x4 w1[14];
#pragma unroll
    for (int s = 0; s < 14; ++s) w1[s] = ldwf(rs, l16, OFF_WQKV + (h1 * 6 + nt6) * 14 * KB, s);

#pragma unroll 1
    for (int l = 0; l < 2; ++l) {
      const unsigned st = 5 * l;
      u32x4 w3[16], w4[28], w5[32];
      touch<14>(w1);
#define PRE34(k0, k1)                                                                                   \
  SCHED();                                                                                              \
  _Pragma("unroll") for (int k = k0; k < k1; ++k) {                                                     \
    if (k < 16) w3[k] = ldwf(rs, l16, OFF_WO + J3 * 16 * KB, k);                                        \
    else w4[k - 16] = ldwf(rs, l16, OFF_WGU + J4 * 28 * KB, k - 16);                                    \
  }                                                                                                     \
  SCHED();
      auto half1 = [&](auto x_t) {
        constexpr int X = decltype(x_t)::value;
        IDS();
        if (wave < 6) {
          lwait(fl, F_RDY + X, st + 1);
          STAMP();
          const char* xs = sm + X * L_REG + (g >> 1) * 1024 + half_off;
          const char* stp = sm + L_ST + X * 512;
          const unsigned so_kv = OFF_KV + l * KV_BYTES + (h1 * 64 + X * 32 + 2 * c1) * 4096;
          f32x4 acc[4];
          u32x4 xf[14][4];
          unsigned sfl;
#pragma unroll
          for (int t = 0; t < 4; ++t) acc[t] = f32x4{0.f, 0.f, 0.f, 0.f};
#define P1_X(s, t) lds16(xs + ((t) >> 1) * 28 * 1024 + (s) * 2048 + ((t) & 1) * 512)
#define P1_LOOP(MM)                                                                                     \
  _Pragma("unroll") for (int s = 0; s < 2; ++s) {                                                       \
    _Pragma("unroll") for (int t = 0; t < 4; ++t) xf[s][t] = P1_X(s, t);                                \
  }                                                                                                     \
  _Pragma("unroll") for (int s = 0; s < 14; ++s) {                                                      \
    if (s + 2 < 14) { _Pragma("unroll") for (int t = 0; t < 4; ++t) xf[s + 2][t] = P1_X(s + 2, t); }    \
    if (s == 12) { sfl = fl[F_SRDY + X]; asm volatile("" ::: "memory"); P1_EPI_LOADS }                  \
    SCHED();                                                                                            \
    _Pragma("unroll") for (int t = 0; t < 4; ++t) acc[t] = MM;                                          \
    SCHED();                                                                                            \
  }                                                                                                     \
  if ((unsigned)__builtin_amdgcn_readfirstlane(sfl) < st + 1) { lwait(fl, F_SRDY + X, st + 1); P1_EPI_LOADS }
          if (part < 2) {
            f32x4 cs1; f32x2 m[4];
#define P1_EPI_LOADS cs1 = *(const f32x4*)(sm + L_CS + (nt6 * 16 + 4 * g) * 4); _Pragma("unroll") for (int t = 0; t < 4; ++t) m[t] = *(const f32x2*)(stp + t * 128 + i * 8);
            P1_LOOP(mfma(w1[s], xf[s][t], acc[t]))
#undef P1_EPI_LOADS
            STAMP();
            PRIO(2);
            const float sc = part == 0 ? 0.18898223650461361f * 1.4426950408889634f : 1.f;
            float v[16];
#pragma unroll
            for (int k = 0; k < 16; ++k) { v[k] = __builtin_fmaf(-m[k >> 2].x, cs1[k & 3], acc[k >> 2][k & 3]); OPQ(v[k]); }
#pragma unroll
            for (int t = 0; t < 4; ++t) m[t].y *= sc;
            SCHED();
#pragma unroll
            for (int k = 0; k < 16; ++k) { v[k] *= m[k >> 2].y; OPQ(v[k]); }
            SCHED();
            const u32x4 o0 = pair16(u32x2{pk(v[0], v[1]), pk(v[2], v[3])}, u32x2{pk(v[4], v[5]), pk(v[6], v[7])});
            const u32x4 o1 = pair16(u32x2{pk(v[8], v[9]), pk(v[10], v[11])}, u32x2{pk(v[12], v[13]), pk(v[14], v[15])});
            const unsigned off = (g >> 1) * 1024 + nt1 * 512 + half_off;
            if (part == 0) {
              *(u32x4*)(sm + X * L_REG + L_Q + off) = o0;
              *(u32x4*)(sm + X * L_REG + L_Q + 2048 + off) = o1;
            } else {
              sth(o0, ws, off, so_kv); sth(o1, ws, off, so_kv + 4096);
              *(u32x4*)(sm + X * L_REG + L_OWN + off) = o0;
              *(u32x4*)(sm + X * L_REG + L_OWN + 4096 + off) = o1;
            }
          } else {
            float c; f32x4 ma[4], mb[4];
#define P1_EPI_LOADS c = *(const float*)(sm + L_CS + (nt6 * 16 + i) * 4); _Pragma("unroll") for (int t = 0; t < 4; ++t) { ma[t] = *(const f32x4*)(stp + t * 128 + g * 32); mb[t] = *(const f32x4*)(stp + t * 128 + g * 32 + 16); }
            P1_LOOP(mfma(xf[s][t], w1[s], acc[t]))
#undef P1_EPI_LOADS
            STAMP();
            PRIO(2);
            float v[16];
#pragma unroll
            for (int k = 0; k < 16; ++k) { const f32x4 mm = (k & 2) ? mb[k >> 2] : ma[k >> 2]; v[k] = __builtin_fmaf(-mm[2 * (k & 1)], c, acc[k >> 2][k & 3]); OPQ(v[k]); }
            SCHED();
#pragma unroll
            for (int k = 0; k < 16; ++k) { const f32x4 mm = (k & 2) ? mb[k >> 2] : ma[k >> 2]; v[k] *= mm[2 * (k & 1) + 1]; OPQ(v[k]); }
            SCHED();
            u32x4 o0 = {pk(v[0], v[1]), pk(v[2], v[3]), pk(v[4], v[5]), pk(v[6], v[7])};
            u32x4 o1 = {pk(v[8], v[9]), pk(v[10], v[11]), pk(v[12], v[13]), pk(v[14], v[15])};
            if (nt1 == 1 && i == 12) { o0 = u32x4{0x3f803f80u, 0x3f803f80u, 0x3f803f80u, 0x3f803f80u}; o1 = o0; }
            const unsigned off = 2048 + nt1 * 1024 + l16;
            sth(o0, ws, off, so_kv); sth(o1, ws, off, so_kv + 4096);
            *(u32x4*)(sm + X * L_REG + L_OWN + off) = o0;
            *(u32x4*)(sm + X * L_REG + L_OWN + 4096 + off) = o1;
          }
          arrive_nr(fl, F_ARR1 + X);
          STAMP();
        }
        PRE34(4 * X, 4 * X + 4)
        if (wave < 4) {
          WAIT_W(4);
          gadd(ws, FLG_KV + ((l * 16 + h1) * 32 + X * 16 + c1) * 128);
          STAMP();
        }
        PRIO(0);
      };
      half1(std::integral_constant<int, 0>{});
      half1(std::integral_constant<int, 1>{});
      auto half2 = [&](auto x_t) {
        constexpr int X = decltype(x_t)::value;
        IDS();
        const int T = X * 64 + 4 * c1 + wave;
        lwait(fl, F_RDY + X, st + 2);
        if (wave < 4) {
          STAMP();
          const u32x4 q = lds16(sm + X * L_REG + L_Q + wave * 1024 + l16);
          const char* kv = sm + X * L_REG + L_HALO + (wave >> 1) * 4096 + l16;
          const int nskip = 8 - 2 * (X * 16 + c1) - (wave >> 1);
          u32x4 o;
          if (X == 0 && nskip > 0) o = attn_tile<true>(kv, q, wave, nskip, lane);
          else o = attn_tile<false>(kv, q, wave, 0, lane);
          sth(o, ws, l16, OFF_O + l * O_BYTES + (T * 16 + h1) * KB);
          arrive_nr(fl, F_ARR2 + X);
          STAMP();
        }
        PRE34(8 + 4 * X, 12 + 4 * X)
        if (wave < 4) {
          WAIT_W(4);
          gadd(ws, CNT_O + (l * 128 + T) * 128);
          STAMP();
        }
      };
      half2(std::integral_constant<int, 0>{});
      half2(std::integral_constant<int, 1>{});
      auto half3 = [&](auto x_t) {
        constexpr int X = decltype(x_t)::value;
        IDS();
        lwait(fl, F_RDY + X, st + 3);
        STAMP();
        const char* os = sm + X * L_REG + l16;
        u32x4 of[16];
#pragma unroll
        for (int s = 0; s < 14; ++s) of[s] = lds16(os + s * 1024);
        const u32x2 xr = *(const u32x2*)(sm + L_RES + X * 4096 + wave * 512 + res_off);
        f32x4 a0 = {0.f, 0.f, 0.f, 0.f}, a1 = {0.f, 0.f, 0.f, 0.f};
#pragma unroll
        for (int s = 0; s < 16; s += 2) {
          SCHED();
          a0 = mfma(w3[s], of[s], a0); a1 = mfma(w3[s + 1], of[s + 1], a1);
          SCHED();
          if (s + 14 < 16) { of[s + 14] = lds16(os + (s + 14) * 1024); of[s + 15] = lds16(os + (s + 15) * 1024); }
        }
        PRIO(2);
        float v0 = a0[0] + a1[0] + bflo(xr.x), v1 = a0[1] + a1[1] + bfhi(xr.x);
        float v2 = a0[2] + a1[2] + bflo(xr.y), v3 = a0[3] + a1[3] + bfhi(xr.y);
        u32x2 pv = {pk(v0, v1), pk(v2, v3)};
        u32x4 o = pair16(pv, pv);
        const int T = X * 64 + 4 * c3 + (m3 >> 2);
        if (lane < 32) sth(o, ws, half_off, OFF_X + (2 * l + 1) * X_BYTES + ((T >> 1) * 28 + J3) * KB + (T & 1) * 512);
        float s1 = (v0 + v1) + (v2 + v3), s2 = (v0 * v0 + v1 * v1) + (v2 * v2 + v3 * v3);
        auto r = __builtin_amdgcn_permlane32_swap(f2u(s1), f2u(s2), false, false);
        float sa = u2f(r[0]) + u2f(r[1]);
        auto t = __builtin_amdgcn_permlane16_swap(f2u(sa), f2u(sa), false, false);
        sa = u2f(t[0]) + u2f(t[1]);
        sth32(f2u(sa), ws, (g >> 1) * 64 + i * 4, OFF_ST + (2 * l + 1) * ST_BYTES + (T * 28 + J3) * 128);
        STAMP();
        PRE34(16 + 4 * X, 20 + 4 * X)
        WAIT_W(4);
        if (arrive(fl, F_ARR3 + X, 7 * (l + 1))) gadd(ws, CNT_X1 + (l * 64 + (T >> 1)) * 128);
        PRIO(0);
        STAMP();
      };
      half3(std::integral_constant<int, 0>{});
      half3(std::integral_constant<int, 1>{});
      auto half4 = [&](auto x_t) {
        constexpr int X = decltype(x_t)::value;
        IDS();
        lwait(fl, F_RDY + X, st + 4);
        STAMP();
        PRIO(1 - X);
        const char* xs = sm + X * L_REG + f32_off;
        const char* stp = sm + L_ST + X * 512 + (lane & 31) * 8;
        const char* csp = sm + L_CS + (96 + wave * 16 + 4 * (lane >> 5)) * 4;
        f32x16 a0, a1;
#pragma unroll
        for (int r = 0; r < 16; ++r) { a0[r] = 0.f; a1[r] = 0.f; }
        u32x4 xa[28], xb[28];
        f32x2 m0, m1; f32x4 cg0, cg1, cu0, cu1;
        unsigned sfl;
#define P4_LOOP(STREAM)                                                                                 \
  _Pragma("unroll") for (int s = 0; s < 2; ++s) { xa[s] = lds16(xs + s * 1024); xb[s] = lds16(xs + (28 + s) * 1024); } \
  _Pragma("unroll") for (int s = 0; s < 28; ++s) {                                                      \
    if (s + 2 < 28) { xa[s + 2] = lds16(xs + (s + 2) * 1024); xb[s + 2] = lds16(xs + (30 + s) * 1024); } \
    if (s == 26) { sfl = fl[F_SRDY + X]; asm volatile("" ::: "memory"); m0 = *(const f32x2*)stp; m1 = *(const f32x2*)(stp + 256); \
                   cg0 = *(const f32x4*)csp; cg1 = *(const f32x4*)(csp + 32); }                         \
    if (s == 27) { cu0 = *(const f32x4*)(csp + 448); cu1 = *(const f32x4*)(csp + 480); }                \
    if (STREAM && s < 10) { w4[8 + 2 * s] = ldwf(rs, l16, OFF_WGU + J4 * 28 * KB, 8 + 2 * s);           \
                            w4[9 + 2 * s] = ldwf(rs, l16, OFF_WGU + J4 * 28 * KB, 9 + 2 * s); }         \
    SCHED();                                                                                            \
    a0 = mfma32(w4[s], xa[s], a0); a1 = mfma32(w4[s], xb[s], a1);                                       \
    SCHED();                                                                                            \
  }
        if (X == 0) {
          if (wave < 4) { P4_LOOP(true) }
          else { PRE34(24, 44) P4_LOOP(false) }
        } else {
          P4_LOOP(false)
        }
        if ((unsigned)__builtin_amdgcn_readfirstlane(sfl) < st + 4) {
          lwait(fl, F_SRDY + X, st + 4);
          m0 = *(const f32x2*)stp; m1 = *(const f32x2*)(stp + 256);
        }
        STAMP();
        PRIO(2);
        const unsigned so = OFF_H + l * H_BYTES + ((X * 32 + 2 * c1) * 112 + J4) * KB;
        sth(geglu8(a0, m0, cg0, cg1, cu0, cu1), ws, f32_off, so);
        sth(geglu8(a1, m1, cg0, cg1, cu0, cu1), ws, f32_off, so + 112 * KB);
        STAMP();
        SCHED();
        const unsigned so5 = OFF_WD + ((act5 ? ng : 0) * 112 + 16 * wave) * 2 * KB;
#pragma unroll
        for (int k = 0; k < 16; ++k) w5[16 * X + k] = ldwf(rs, l16, so5, 16 * X + k);
        SCHED();
        WAIT_W(16);
        if (arrive(fl, F_ARR4 + X, 7 * (l + 1))) gadd(ws, CNT_H + (l * 32 + X * 16 + c1) * 128);
        PRIO(0);
        STAMP();
      };
      half4(std::integral_constant<int, 0>{});
      half4(std::integral_constant<int, 1>{});
      SCHED();
      u32x4 hf[8];
      auto load5 = [&](int X) {
        IDS();
        const unsigned so = OFF_H + l * H_BYTES + ((X * 32 + 2 * c3 + th) * 112 + 16 * wave + 8) * KB;
#pragma unroll
        for (int s = 0; s < 8; ++s) hf[s] = ldhf(rs, f32_off, so, s);
      };
      auto mma5 = [&](int X) {
        IDS();
        lwait(fl, F_SRDY + X, st + 5);
        char* mine = sm + X * L_REG + wave * 8192 + l16;
        u32x4 hl[8];
#pragma unroll
        for (int s = 0; s < 8; ++s) hl[s] = lds16(mine + s * 1024);
        f32x16 a0, a1;
#pragma unroll
        for (int r = 0; r < 16; ++r) { a0[r] = 0.f; a1[r] = 0.f; }
#pragma unroll
        for (int s = 0; s < 16; ++s) {
          SCHED();
          a0 = mfma32(w5[2 * s], s < 8 ? hl[s] : hf[s - 8], a0); a1 = mfma32(w5[2 * s + 1], s < 8 ? hl[s] : hf[s - 8], a1);
        }
        SCHED();
#pragma unroll
        for (int k = 0; k < 4; ++k) {
          *(f32x4*)(mine + k * 1024) = f32x4{a0[4 * k], a0[4 * k + 1], a0[4 * k + 2], a0[4 * k + 3]};
          *(f32x4*)(mine + (4 + k) * 1024) = f32x4{a1[4 * k], a1[4 * k + 1], a1[4 * k + 2], a1[4 * k + 3]};
        }
        arrive_nr(fl, F_RED + X);
      };
      auto fin5 = [&](int X) {
        IDS();
        PRIO(2);
        const u32x4 xr = lds16(sm + L_RES + X * 4096 + wave * 1024 + l16);
        lwait(fl, F_RED + X, 7 * (l + 1));
        const char* red = sm + X * L_REG + wave * 2048 + l16;
        f32x4 pa[7], pb[7];
#pragma unroll
        for (int k = 0; k < 7; ++k) { pa[k] = *(const f32x4*)(red + k * 8192); pb[k] = *(const f32x4*)(red + k * 8192 + 1024); }
        float v[8] = {bflo(xr.x), bfhi(xr.x), bflo(xr.y), bfhi(xr.y), bflo(xr.z), bfhi(xr.z), bflo(xr.w), bfhi(xr.w)};
        SCHED();
#pragma unroll
        for (int k = 0; k < 7; ++k) {
#pragma unroll
          for (int r = 0; r < 8; ++r) { v[r] += r < 4 ? pa[k][r] : pb[k][r - 4]; OPQ(v[r]); }
          SCHED();
        }
        const u32x4 o = {pks(v[0], v[1]), pks(v[2], v[3]), pks(v[4], v[5]), pks(v[6], v[7])};
        const int P = X * 32 + 2 * c3 + th, J = 4 * ng + wave;
        if (l == 0) {
          sth(o, ws, f32_off, OFF_X + 2 * X_BYTES + (P * 28 + J) * KB);
          float s1 = v[0] + v[1], s2 = v[0] * v[0], s3 = v[2] + v[3], s4 = v[1] * v[1];
          OPQ(s1); OPQ(s2); OPQ(s3); OPQ(s4);
          s1 += v[4]; s3 += v[5]; s2 = __builtin_fmaf(v[2], v[2], s2); s4 = __builtin_fmaf(v[3], v[3], s4);
          OPQ(s1); OPQ(s2); OPQ(s3); OPQ(s4);
          s1 += v[6]; s3 += v[7]; s2 = __builtin_fmaf(v[4], v[4], s2); s4 = __builtin_fmaf(v[5], v[5], s4);
          OPQ(s1); OPQ(s2); OPQ(s3); OPQ(s4);
          s1 += s3; s2 = __builtin_fmaf(v[6], v[6], s2); s4 = __builtin_fmaf(v[7], v[7], s4);
          OPQ(s1); OPQ(s2); OPQ(s4);
          s2 += s4;
          auto r = __builtin_amdgcn_permlane32_swap(f2u(s1), f2u(s2), false, false);
          const float sv = u2f(r[0]) + u2f(r[1]);
          sth32(f2u(sv), ws, (g & 1) * 3584 + (lane >> 5) * 64 + i * 4, OFF_ST + 2 * ST_BYTES + (2 * P * 28 + J) * 128);
        } else {
          uint16_t* op = out + (size_t)(P * 32 + (lane & 31)) * D_ + 16 * J + 4 * (lane >> 5);
          *(u32x2*)op = u32x2{o.x, o.y};
          *(u32x2*)(op + 8) = u32x2{o.z, o.w};
        }
      };
      auto sig5 = [&](int X) {
        IDS();
        if (arrive(fl, F_ARR5 + X, 4 * (l + 1))) { if (l == 0) gadd(ws, CNT_X2 + (X * 32 + 2 * c3 + th) * 128); }
        PRIO(0);
      };
      if (act5) {
        lwait(fl, F_RDY + 0, st + 5);
        STAMP();
        load5(0);
        SCHED();
        mma5(0);
        STAMP();
        bool early = true;
        if (wave0 < 4) {
          fin5(0);
          STAMP();
          early = lready(fl, F_RDY + 1, st + 5);
          if (!early) { WAIT_ALL(); sig5(0); }
        }
        lwait(fl, F_RDY + 1, st + 5);
        STAMP();
        SCHED();
        load5(1);
        SCHED();
        if (wave0 < 4 && early) { asm volatile("s_waitcnt vmcnt(8) lgkmcnt(0)" ::: "memory"); sig5(0); }
        SCHED();
        mma5(1);
        STAMP();
        if (wave0 < 4) fin5(1);
        STAMP();
      }
      SCHED();
      {
        IDS();
#pragma unroll
        for (int s = 0; s < 14; ++s) w1[s] = ldwf(rs, l16, OFF_WQKV + (h1 * 6 + nt6) * 14 * KB, s);
      }
      SCHED();
      if (act5 && wave0 < 4) {
        WAIT_W(14);
        sig5(1);
        STAMP();
      }
    }
  }
}

extern "C" void kernel_launch(void* const* d_in, const int* in_sizes, int n_in, void* d_out, int out_size, void* d_ws,
                              size_t ws_size, hipStream_t stream) {
  const uint16_t* x = (const uint16_t*)d_in[0];
  const uint16_t* wqkv = (const uint16_t*)d_in[1];
  const uint16_t* wo = (const uint16_t*)d_in[2];
  const uint16_t* wg = (const uint16_t*)d_in[4];
  const uint16_t* wu = (const uint16_t*)d_in[5];
  const uint16_t* wd = (const uint16_t*)d_in[6];
  char* ws = (char*)d_ws;
  (void)hipFuncSetAttribute((const void*)k_main, hipFuncAttributeMaxDynamicSharedMemorySize, L_BYTES);
  k_prep<<<256, NT, 0, stream>>>(x, wqkv, wo, wg, wu, wd, ws);
  k_main<<<256, NT, L_BYTES, stream>>>(ws, (uint16_t*)d_out);
}
```

```cpp
#include <hip/hip_runtime.h>
#include <hip/hip_bf16.h>
#include <stdint.h>
#include <type_traits>

typedef short bf16x8 __attribute__((ext_vector_type(8)));
typedef float f32x4 __attribute__((ext_vector_type(4)));
typedef float f32x2 __attribute__((ext_vector_type(2)));
typedef float f32x16 __attribute__((ext_vector_type(16)));
typedef unsigned u32x4 __attribute__((ext_vector_type(4)));
typedef unsigned u32x2 __attribute__((ext_vector_type(2)));
typedef __bf16 bf16x2_t __attribute__((ext_vector_type(2)));
#define GAS __attribute__((address_space(1)))
#define LAS __attribute__((address_space(3)))
typedef GAS unsigned gu32;
typedef volatile LAS unsigned* lflag;
#define RLX_AGENT __ATOMIC_RELAXED, __HIP_MEMORY_SCOPE_AGENT

constexpr int D_ = 448, DH = 28, F_ = 1792;
constexpr int NT = 512;
constexpr unsigned KB = 1024;

constexpr unsigned CNT_X2 = 0;
constexpr unsigned CNT_O  = CNT_X2 + 64 * 128;
constexpr unsigned CNT_X1 = CNT_O + 256 * 128;
constexpr unsigned CNT_H  = CNT_X1 + 128 * 128;
constexpr unsigned CNT_KV = CNT_H + 64 * 128;
constexpr unsigned NCTRL  = CNT_KV / 128 + 512;
static_assert(NCTRL == 1024, "k_prep clears four lines per workgroup");
constexpr unsigned OFF_WQKV = 128 * KB;
constexpr unsigned OFF_WO   = OFF_WQKV + 16 * 6 * 14 * KB;
constexpr unsigned OFF_WGU  = OFF_WO + 28 * 16 * KB;
constexpr unsigned OFF_WD   = OFF_WGU + 112 * 28 * KB;
constexpr unsigned OFF_CS   = OFF_WD + 7 * 56 * 4 * KB;
constexpr unsigned X_BYTES  = 128 * 28 * 512;
constexpr unsigned OFF_X    = OFF_CS + 32 * KB;
constexpr unsigned ST_BYTES = 128 * 28 * 128;
constexpr unsigned OFF_ST   = OFF_X + 5 * X_BYTES;
constexpr unsigned KV_BYTES = 16 * 64 * 4 * KB;
constexpr unsigned OFF_KV   = OFF_ST + 4 * ST_BYTES;
constexpr unsigned O_BYTES  = 128 * 16 * KB;
constexpr unsigned OFF_O    = OFF_KV + 2 * KV_BYTES;
constexpr unsigned H_BYTES  = 128 * 56 * KB;
constexpr unsigned OFF_H    = OFF_O + 2 * O_BYTES;
constexpr unsigned WS_USED  = OFF_H + 2 * H_BYTES;
constexpr unsigned OFF_STAMP = 64u << 20;

constexpr int L_REG = 80 * 1024;
constexpr int L_HALO = 24 * 1024, L_OWN = 56 * 1024, L_Q = 72 * 1024;
constexpr int L_RES = 136 * 1024;
constexpr int L_ST = L_RES + 8192;
constexpr int L_CS = L_ST + 1024;
constexpr int L_FL = L_CS + 2048;
constexpr int L_STAMP = L_FL + 256;
constexpr int L_BYTES = L_STAMP + 8 * 128 * 4;
enum { F_RDY = 0, F_SRDY = 2, F_ARR1 = 4, F_ARR3 = 5, F_ARR4 = 6, F_ARR5 = 8, F_RED = 10, F_ABORT = 12 };

__device__ __forceinline__ f32x4 mfma(u32x4 a, u32x4 b, f32x4 c) {
  return __builtin_amdgcn_mfma_f32_16x16x32_bf16(__builtin_bit_cast(bf16x8, a), __builtin_bit_cast(bf16x8, b), c, 0, 0, 0);
}
__device__ __forceinline__ f32x16 mfma32(u32x4 a, u32x4 b, f32x16 c) {
  return __builtin_amdgcn_mfma_f32_32x32x16_bf16(__builtin_bit_cast(bf16x8, a), __builtin_bit_cast(bf16x8, b), c, 0, 0, 0);
}
__device__ __forceinline__ unsigned pk(float a, float b) {
  bf16x2_t v = {(__bf16)a, (__bf16)b};
  return __builtin_bit_cast(unsigned, v);
}
__device__ __forceinline__ unsigned pks(float a, float b) {
  unsigned r;
  asm("v_cvt_pk_bf16_f32 %0, %1, %2" : "=v"(r) : "v"(a), "v"(b));
  return r;
}
__device__ __forceinline__ float bflo(unsigned u) { return __builtin_bit_cast(float, u << 16); }
__device__ __forceinline__ float bfhi(unsigned u) { return __builtin_bit_cast(float, u & 0xffff0000u); }
__device__ __forceinline__ unsigned f2u(float f) { return __builtin_bit_cast(unsigned, f); }
__device__ __forceinline__ float u2f(unsigned u) { return __builtin_bit_cast(float, u); }
__device__ __forceinline__ u32x4 lds16(const char* p) { return *(const u32x4*)p; }
#define FW(a, b) (a)
#define SCHED() __builtin_amdgcn_sched_barrier(0)
#define OPQ(x) asm("" : "+v"(x))
#define PRIO(n) __builtin_amdgcn_s_setprio(n)
#define WAIT_VM(n) asm volatile("s_waitcnt vmcnt(" #n ")" ::: "memory")
#define WAIT_ALL() asm volatile("s_waitcnt vmcnt(0) lgkmcnt(0)" ::: "memory")
#define WAIT_LGKM() asm volatile("s_waitcnt lgkmcnt(0)" ::: "memory")
typedef __amdgpu_buffer_rsrc_t rsrc_t;
__device__ __forceinline__ u32x4 ldw(rsrc_t rs, unsigned voff, unsigned soff) { return __builtin_amdgcn_raw_buffer_load_b128(rs, voff, soff, 0); }
__device__ __forceinline__ u32x4 ldh(rsrc_t rs, unsigned voff, unsigned soff) { return __builtin_amdgcn_raw_buffer_load_b128(rs, voff, soff, 16); }
__device__ __forceinline__ u32x4 ldwf(rsrc_t rs, unsigned l16, unsigned base, int k) { return ldw(rs, l16 + (k & 3) * 1024, base + (k >> 2) * 4096); }
#define WCNT(n) #n
#define WAIT_W(n) asm volatile("s_waitcnt vmcnt(" WCNT(n) ") lgkmcnt(0)" ::: "memory")
__device__ __forceinline__ u32x4 ldhf(rsrc_t rs, unsigned l16, unsigned base, int k) { return ldh(rs, l16 + (k & 3) * 1024, base + (k >> 2) * 4096); }
__device__ __forceinline__ unsigned opaque(unsigned v) { asm volatile("" : "+s"(v)); return v; }
template <int N> __device__ __forceinline__ void touch(const u32x4* w) {
#pragma unroll
  for (int k = 0; k < N; ++k) asm volatile("" :: "v"(w[k]));
}
template <int N> __device__ __forceinline__ void undef(u32x4* w) {
#pragma unroll
  for (int k = 0; k < N; ++k) asm volatile("" : "=v"(w[k]));
}
__device__ __forceinline__ int opaque_v(int v) { asm volatile("" : "+v"(v)); return v; }
__device__ __forceinline__ void sth(u32x4 v, char* ws, unsigned voff, unsigned soff) {
  asm volatile("global_store_dwordx4 %0, %1, %2 sc1\n\ts_nop 1" :: "v"(voff + soff), "v"(v), "s"(ws) : "memory");
}
__device__ __forceinline__ void sth32(unsigned v, char* ws, unsigned voff, unsigned soff) {
  asm volatile("global_store_dword %0, %1, %2 sc1" :: "v"(voff + soff), "v"(v), "s"(ws) : "memory");
}

#define STAMP() do {} while (0)

typedef short s16x4 __attribute__((ext_vector_type(4)));
__device__ __forceinline__ u32x2 ldtr(const char* p) {
  return __builtin_bit_cast(u32x2, __builtin_amdgcn_ds_read_tr16_b64_v4i16((LAS s16x4*)(LAS char*)p));
}
__device__ __forceinline__ float rbf(float v) { return bflo(pk(v, v) & 0xffffu); }
__device__ __forceinline__ float sum8(u32x4 v) {
  return ((bflo(v.x) + bfhi(v.x)) + (bflo(v.y) + bfhi(v.y))) + ((bflo(v.z) + bfhi(v.z)) + (bflo(v.w) + bfhi(v.w)));
}
__device__ __forceinline__ float wave_sum(float v) {
#pragma unroll
  for (int o = 1; o < 64; o <<= 1) v += __shfl_xor(v, o);
  return v;
}

constexpr int PW_STRIDE = 80, PW_BYTES = 452 * PW_STRIDE, PX_OFF = PW_BYTES, PR_OFF = PX_OFF + 2 * 7168;
__global__ __launch_bounds__(NT) void k_prep(const uint16_t* __restrict__ x, const uint16_t* __restrict__ wqkv,
                                             const uint16_t* __restrict__ wo, const uint16_t* __restrict__ wg,
                                             const uint16_t* __restrict__ wu, const uint16_t* __restrict__ wd, char* ws) {
  __shared__ __attribute__((aligned(16))) char sm[PR_OFF + 8 * 32 * 4];
  const int b = blockIdx.x, tid = threadIdx.x, lane = tid & 63, wave = __builtin_amdgcn_readfirstlane(tid >> 6);
  if (tid < 4) *(unsigned*)(ws + (b * 4 + tid) * 128) = 0u;
  const u32x4 xv = *(const u32x4*)(x + (size_t)(b * 8 + wave) * D_ + min(lane, 55) * 8);
  const uint16_t* src = wo; int ld = D_, ncols = 32, type = 3; char* dst = ws; int csf = -1, csb = 0, s_stride = 0, nt_stride = 0;
  if (b < 48) {
    int h = b / 3, p = b % 3; type = 0;
    src = wqkv + p * D_ + DH * h; ld = 3 * D_; ncols = DH;
    dst = ws + OFF_WQKV + (size_t)(h * 3 + p) * 28 * KB; s_stride = 1024; nt_stride = 14 * 1024;
    csf = h * 112 + p * 32;
  } else if (b < 62) {
    int j = b - 48; type = 1;
    src = wo + 32 * j;
    dst = ws + OFF_WO + (size_t)(2 * j) * 16 * KB; s_stride = 1024; nt_stride = 16 * 1024;
  } else if (b < 174) {
    int j = (b - 62) >> 1, u = (b - 62) & 1; type = 2;
    src = (u ? wu : wg) + 32 * j; ld = F_;
    dst = ws + OFF_WGU + (size_t)(2 * j) * 28 * KB + u * 256; s_stride = 1024; nt_stride = 28 * 1024;
    csf = 32 * j; csb = 96 + 112 * u;
  } else if (b < 230) {
    int j = (b - 174) >> 2, q = (b - 174) & 3; type = 2;
    src = wd + (size_t)q * 448 * D_ + 32 * j;
    dst = ws + OFF_WD + ((size_t)(j >> 1) * 112 + q * 28) * 2 * KB + (j & 1) * KB; s_stride = 2048; nt_stride = 256;
  }
  u32x2 wv[7];
  const int c4 = tid & 7;
  const bool cok = c4 * 4 < ncols;
#pragma unroll
  for (int u = 0; u < 7; ++u) wv[u] = *(const u32x2*)(src + (size_t)(u * 64 + (tid >> 3)) * ld + (cok ? c4 * 4 : 0));
  {
    const float e[8] = {bflo(xv.x), bfhi(xv.x), bflo(xv.y), bfhi(xv.y), bflo(xv.z), bfhi(xv.z), bflo(xv.w), bfhi(xv.w)};
    const float mean = wave_sum(lane < 56 ? sum8(xv) : 0.f) * (1.f / D_);
    float q = 0.f;
#pragma unroll
    for (int k = 0; k < 8; ++k) q += (e[k] - mean) * (e[k] - mean);
    const float var = wave_sum(lane < 56 ? q : 0.f) * (1.f / D_);
    const float mb = rbf(mean), sd = rbf(__builtin_sqrtf(rbf(rbf(var) + 1e-5f)));
    float y[8];
#pragma unroll
    for (int k = 0; k < 8; ++k) y[k] = rbf(e[k] - mb) / sd;
    if (lane < 56) {
      char* d = sm + PX_OFF + (lane >> 1) * 256 + wave * 16 + (lane & 1) * 8;
      *(u32x2*)d = u32x2{xv.x, xv.y}; *(u32x2*)(d + 128) = u32x2{xv.z, xv.w};
      *(u32x2*)(d + 7168) = u32x2{pk(y[0], y[1]), pk(y[2], y[3])}; *(u32x2*)(d + 7168 + 128) = u32x2{pk(y[4], y[5]), pk(y[6], y[7])};
    }
  }
#pragma unroll
  for (int u = 0; u < 7; ++u) *(u32x2*)(sm + (u * 64 + (tid >> 3)) * PW_STRIDE + c4 * 8) = cok ? wv[u] : u32x2{0u, 0u};
  if (tid < 40) *(u32x2*)(sm + 448 * PW_STRIDE + tid * 8) = u32x2{0u, 0u};
  __syncthreads();
  if (tid < 448) {
    const unsigned o = ((b >> 2) * 28 + (tid >> 4)) * KB + ((b >> 1) & 1) * 512 + ((tid >> 3) & 1) * 256 + (b & 1) * 128 + (tid & 7) * 16;
    *(u32x4*)(ws + OFF_X + o) = lds16(sm + PX_OFF + tid * 16);
    *(u32x4*)(ws + OFF_X + 4 * X_BYTES + o) = lds16(sm + PX_OFF + 7168 + tid * 16);
  }
  if (type == 3) return;
  const int i = lane & 15, g = lane >> 4, q4 = i >> 2, p4 = i & 3;
  float csum = 0.f;
#pragma unroll
  for (int k = 0; k < 4; ++k) {
    const int f = wave + 8 * k;
    if (f < (type == 1 ? 32 : 28)) {
      int s, nt, r0, r1;
      if (type == 2) { s = f; nt = g & 1; r0 = 16 * s + 4 * (g >> 1); r1 = r0 + 8; }
      else if (type == 0) { s = f >> 1; nt = f & 1; r0 = 32 * s + 16 * (g >> 1) + 4 * (g & 1); r1 = r0 + 8; }
      else { s = f >> 1; nt = f & 1; r0 = DH * s + 4 * g; r1 = g < 3 ? r0 + 16 : 448; }
      const u32x2 lo = ldtr(sm + (r0 + q4) * PW_STRIDE + nt * 32 + p4 * 8), hi = ldtr(sm + (r1 + q4) * PW_STRIDE + nt * 32 + p4 * 8);
      const u32x4 o = {lo.x, lo.y, hi.x, hi.y};
      *(u32x4*)(dst + (size_t)s * s_stride + (size_t)nt * nt_stride + (type == 2 ? i * 16 + (g >> 1) * 512 : lane * 16)) = o;
      csum += sum8(o);
    }
  }
  if (csf >= 0) {
    csum += __shfl_xor(csum, 32);
    if (type == 0) csum += __shfl_xor(csum, 16);
    float* red = (float*)(sm + PR_OFF);
    if (lane < 32) red[wave * 32 + lane] = (type == 0 && (lane >> 4) != (wave & 1)) ? 0.f : csum;
    __syncthreads();
    if (tid < 32) {
      float t = 0.f;
#pragma unroll
      for (int w = 0; w < 8; ++w) t += red[w * 32 + tid];
      const int f = csf + tid;
      ((float*)(ws + OFF_CS))[(f / 112) * 512 + csb + f % 112] = t;
    }
  }
}

__device__ __forceinline__ void lwait(lflag fl, int idx, unsigned tgt) {
  unsigned v, sv, n = 1u << 16;
  const unsigned addr = (unsigned)(uintptr_t)(fl + idx);
  asm volatile("1:\n\tds_read_b32 %0, %3\n\ts_waitcnt lgkmcnt(0)\n\tv_readfirstlane_b32 %1, %0\n\ts_cmp_ge_u32 %1, %4\n\t"
               "s_cbranch_scc1 2f\n\ts_sleep 1\n\ts_sub_u32 %2, %2, 1\n\ts_cbranch_scc0 1b\n2:"
               : "=&v"(v), "=&s"(sv), "+s"(n) : "v"(addr), "s"(tgt) : "memory", "scc");
}
__device__ __forceinline__ bool lready(lflag fl, int idx, unsigned tgt) {
  bool r = (unsigned)__builtin_amdgcn_readfirstlane(fl[idx]) >= tgt;
  asm volatile("" ::: "memory");
  return r;
}
__device__ __forceinline__ bool arrive(lflag fl, int idx, unsigned tgt) {
  unsigned old, so;
  const unsigned addr = (unsigned)(uintptr_t)(fl + idx), one = 1u;
  asm volatile("s_mov_b64 exec, 1\n\tds_add_rtn_u32 %0, %2, %3\n\ts_mov_b64 exec, -1\n\ts_waitcnt lgkmcnt(0)\n\tv_readfirstlane_b32 %1, %0"
               : "=&v"(old), "=s"(so) : "v"(addr), "v"(one) : "memory");
  return so + 1u == tgt;
}
__device__ __forceinline__ void arrive_nr(lflag fl, int idx) {
  const unsigned addr = (unsigned)(uintptr_t)(fl + idx), one = 1u;
  asm volatile("s_mov_b64 exec, 1\n\tds_add_u32 %0, %1\n\ts_mov_b64 exec, -1" :: "v"(addr), "v"(one) : "memory");
}
__device__ __forceinline__ void gadd(char* ws, unsigned off) {
  const unsigned one = 1u;
  asm volatile("s_mov_b64 exec, 1\n\tglobal_atomic_add %0, %1, %2\n\ts_mov_b64 exec, -1" :: "v"(off), "v"(one), "s"(ws) : "memory");
}
__device__ __forceinline__ void gpoll(char* ws, unsigned off, unsigned tgt) {
  unsigned v, n = 1u << 15;
  asm volatile("1:\n\tglobal_load_dword %0, %2, %3 sc1\n\ts_waitcnt vmcnt(0)\n\tv_cmp_gt_u32 vcc, %4, %0\n\t"
               "s_cbranch_vccz 2f\n\ts_sub_u32 %1, %1, 1\n\ts_cbranch_scc0 1b\n2:"
               : "=&v"(v), "+s"(n) : "v"(off), "s"(ws), "s"(tgt) : "memory", "scc", "vcc");
}
#define BAR() do { asm volatile("s_waitcnt lgkmcnt(0)" ::: "memory"); __builtin_amdgcn_s_barrier(); asm volatile("" ::: "memory"); } while (0)
__device__ __forceinline__ void dma8k(const char* ws, unsigned voff, unsigned lds) {
  unsigned keep;
  lds = __builtin_amdgcn_readfirstlane(lds + 4096);
  asm volatile("s_mov_b32 %0, m0\n\ts_mov_b32 m0, %3\n\ts_nop 0\n\t"
               "global_load_lds_dwordx4 %1, %2 offset:-4096 sc1\n\tglobal_load_lds_dwordx4 %1, %2 offset:-3072 sc1\n\t"
               "global_load_lds_dwordx4 %1, %2 offset:-2048 sc1\n\tglobal_load_lds_dwordx4 %1, %2 offset:-1024 sc1\n\t"
               "global_load_lds_dwordx4 %1, %2 sc1\n\tglobal_load_lds_dwordx4 %1, %2 offset:1024 sc1\n\t"
               "global_load_lds_dwordx4 %1, %2 offset:2048 sc1\n\tglobal_load_lds_dwordx4 %1, %2 offset:3072 sc1\n\t"
               "s_mov_b32 m0, %0"
               : "=&s"(keep) : "v"(voff + 4096), "s"(ws), "s"(lds) : "memory");
}
__device__ __forceinline__ void dma4k(const char* ws, unsigned voff, unsigned lds) {
  unsigned keep;
  lds = __builtin_amdgcn_readfirstlane(lds);
  asm volatile("s_mov_b32 %0, m0\n\ts_mov_b32 m0, %3\n\ts_nop 0\n\t"
               "global_load_lds_dwordx4 %1, %2 sc1\n\tglobal_load_lds_dwordx4 %1, %2 offset:1024 sc1\n\t"
               "global_load_lds_dwordx4 %1, %2 offset:2048 sc1\n\tglobal_load_lds_dwordx4 %1, %2 offset:3072 sc1\n\t"
               "s_mov_b32 m0, %0"
               : "=&s"(keep) : "v"(voff), "s"(ws), "s"(lds) : "memory");
}
__device__ __forceinline__ void dma1k(const char* ws, unsigned voff, unsigned lds) {
  unsigned keep;
  lds = __builtin_amdgcn_readfirstlane(lds);
  asm volatile("s_mov_b32 %0, m0\n\ts_mov_b32 m0, %3\n\ts_nop 0\n\tglobal_load_lds_dwordx4 %1, %2 sc1\n\ts_mov_b32 m0, %0"
               : "=&s"(keep) : "v"(voff), "s"(ws), "s"(lds) : "memory");
}

constexpr float GC1 = -2.f * 0.7978845608028654f * 1.4426950408889634f, GC2 = GC1 * 0.044715f;
__device__ __forceinline__ u32x4 pair16(u32x2 a, u32x2 b) {
  auto rx = __builtin_amdgcn_permlane32_swap(a.x, b.x, false, false);
  auto ry = __builtin_amdgcn_permlane32_swap(a.y, b.y, false, false);
  return u32x4{rx[0], ry[0], rx[1], ry[1]};
}
__device__ __forceinline__ float allg_max(float m) {
  auto r = __builtin_amdgcn_permlane32_swap(f2u(m), f2u(m), false, false);
  m = fmaxf(u2f(r[0]), u2f(r[1]));
  auto q = __builtin_amdgcn_permlane16_swap(f2u(m), f2u(m), false, false);
  return fmaxf(u2f(q[0]), u2f(q[1]));
}

__device__ __forceinline__ void serve_x(char* ws, char* sm, unsigned smb, lflag fl, int xi, int T0, int X, unsigned stage, int lane) {
  const unsigned src = OFF_X + xi * X_BYTES + (unsigned)T0 * 14 * KB + lane * 16;
#pragma unroll
  for (int k = 0; k < 7; ++k) dma8k(ws, src + k * 8192, smb + X * L_REG + k * 8192);
  if (xi == 4) {
    *(f32x2*)(sm + L_ST + X * 512 + lane * 8) = f32x2{0.f, 1.f};
    WAIT_VM(0);
    fl[F_RDY + X] = stage;
    fl[F_SRDY + X] = stage;
    return;
  }
  u32x4 p[14];
  const unsigned vo = (lane >> 4) * 3584 + (lane & 15) * 16 + OFF_ST + xi * ST_BYTES + (unsigned)T0 * 3584;
  asm volatile("global_load_dwordx4 %0, %14, %15 sc1\n\tglobal_load_dwordx4 %1, %14, %15 offset:256 sc1\n\t"
               "global_load_dwordx4 %2, %14, %15 offset:512 sc1\n\tglobal_load_dwordx4 %3, %14, %15 offset:768 sc1\n\t"
               "global_load_dwordx4 %4, %14, %15 offset:1024 sc1\n\tglobal_load_dwordx4 %5, %14, %15 offset:1280 sc1\n\t"
               "global_load_dwordx4 %6, %14, %15 offset:1536 sc1\n\tglobal_load_dwordx4 %7, %14, %15 offset:1792 sc1\n\t"
               "global_load_dwordx4 %8, %14, %15 offset:2048 sc1\n\tglobal_load_dwordx4 %9, %14, %15 offset:2304 sc1\n\t"
               "global_load_dwordx4 %10, %14, %15 offset:2560 sc1\n\tglobal_load_dwordx4 %11, %14, %15 offset:2816 sc1\n\t"
               "global_load_dwordx4 %12, %14, %15 offset:3072 sc1\n\tglobal_load_dwordx4 %13, %14, %15 offset:3328 sc1\n\t"
               "s_waitcnt vmcnt(14)"
               : "=&v"(p[0]), "=&v"(p[1]), "=&v"(p[2]), "=&v"(p[3]), "=&v"(p[4]), "=&v"(p[5]), "=&v"(p[6]), "=&v"(p[7]), "=&v"(p[8]),
                 "=&v"(p[9]), "=&v"(p[10]), "=&v"(p[11]), "=&v"(p[12]), "=&v"(p[13])
               : "v"(vo), "s"(ws) : "memory");
  fl[F_RDY + X] = stage;
  asm volatile("s_waitcnt vmcnt(0)"
               : "+v"(p[0]), "+v"(p[1]), "+v"(p[2]), "+v"(p[3]), "+v"(p[4]), "+v"(p[5]), "+v"(p[6]), "+v"(p[7]), "+v"(p[8]), "+v"(p[9]),
                 "+v"(p[10]), "+v"(p[11]), "+v"(p[12]), "+v"(p[13]) :: "memory");
  float a[4];
#pragma unroll
  for (int r = 0; r < 4; ++r) {
    float t0 = u2f(p[0][r]) + u2f(p[1][r]), t1 = u2f(p[2][r]) + u2f(p[3][r]);
    OPQ(t0); OPQ(t1);
#pragma unroll
    for (int k = 4; k < 14; k += 2) { t0 += u2f(p[k][r]); t1 += u2f(p[k + 1][r]); OPQ(t0); OPQ(t1); }
    a[r] = t0 + t1;
  }
  float ms[4], rsd[4];
#pragma unroll
  for (int r = 0; r < 4; ++r) {
    float s1 = a[r] + u2f(__builtin_amdgcn_update_dpp(0u, f2u(a[r]), 0x128, 0xf, 0xf, false));
    float s2 = u2f(__builtin_amdgcn_update_dpp(0u, f2u(s1), 0x124, 0xf, 0xf, false));
    float mean = s1 * (1.f / D_);
    OPQ(mean);
    float var = fmaxf(__builtin_fmaf(-mean, mean, s2 * (1.f / D_)), 0.f);
    ms[r] = mean; rsd[r] = __builtin_amdgcn_rsqf(var + 1e-5f);
  }
  if ((lane & 15) < 4) {
    char* d = sm + L_ST + X * 512 + ((lane >> 4) * 16 + (lane & 3) * 4) * 8;
    *(f32x4*)d = f32x4{ms[0], rsd[0], ms[1], rsd[1]};
    *(f32x4*)(d + 16) = f32x4{ms[2], rsd[2], ms[3], rsd[3]};
  }
  asm volatile("" ::: "memory");
  fl[F_SRDY + X] = stage;
}

template <class PRE>
__device__ __forceinline__ u32x4 attn_tile(const char* kv, u32x4 q, int qt, int lane, PRE pre) {
  const int i = lane & 15, g = lane >> 4;
  const float NEG = -__builtin_inff();
  f32x4 s[18];
  u32x4 kf[18];
#pragma unroll
  for (int j = 0; j < 6; ++j) kf[j] = lds16(kv + (j >> 1) * 4096 + (j & 1) * 1024);
#pragma unroll
  for (int j = 0; j < 18; ++j) {
    if (j + 6 < 18) kf[j + 6] = lds16(kv + ((j + 6) >> 1) * 4096 + (j & 1) * 1024);
    SCHED();
    s[j] = mfma(kf[j], q, f32x4{0.f, 0.f, 0.f, 0.f});
    if (j % 4 == 1) pre(j / 4);
    SCHED();
  }
  u32x4 vf[18];
#pragma unroll
  for (int j = 0; j < 4; ++j) vf[j] = lds16(kv + (j >> 1) * 4096 + 2048 + (j & 1) * 1024);
  const int dq = 16 * (qt & 1) + i - 4 * g;
#pragma unroll
  for (int u = 0; u < 2; ++u)
#pragma unroll
    for (int r = 0; r < 4; ++r) {
      const bool lo = dq < 16 * u + r;
      s[u][r] = lo ? s[u][r] : NEG;
      s[16 + u][r] = lo ? NEG : s[16 + u][r];
    }
  float mx[4];
#pragma unroll
  for (int r = 0; r < 4; ++r) mx[r] = fmaxf(s[2][r], s[3][r]);
#pragma unroll
  for (int j = 4; j < 20; j += 2)
#pragma unroll
    for (int r = 0; r < 4; ++r) mx[r] = fmaxf(fmaxf(mx[r], s[j % 18][r]), s[(j + 1) % 18][r]);
  const float m = allg_max(fmaxf(fmaxf(mx[0], mx[1]), fmaxf(mx[2], mx[3])));
  constexpr float SC = 0.18898223650461361f * 1.4426950408889634f;
  const float mc = -m * SC;
  const f32x4 m4 = {mc, mc, mc, mc}, c4 = {SC, SC, SC, SC};
  SCHED();
#pragma unroll
  for (int j = 0; j < 18; ++j) s[j] = __builtin_elementwise_fma(s[j], c4, m4);
  SCHED();
  f32x4 o0 = {0.f, 0.f, 0.f, 0.f}, o1 = {0.f, 0.f, 0.f, 0.f};
#pragma unroll
  for (int j = 0; j < 9; ++j) {
    if (j + 2 < 9) { vf[2 * j + 4] = lds16(kv + (j + 2) * 4096 + 2048); vf[2 * j + 5] = lds16(kv + (j + 2) * 4096 + 3072); }
    float p[8];
#pragma unroll
    for (int k = 0; k < 8; ++k) p[k] = __builtin_amdgcn_exp2f(s[2 * j + (k >> 2)][k & 3]);
    const u32x4 pf = {pk(p[0], p[1]), pk(p[2], p[3]), pk(p[4], p[5]), pk(p[6], p[7])};
    o0 = mfma(vf[2 * j], pf, o0);
    o1 = mfma(vf[2 * j + 1], pf, o1);
    pre(5 + j);
    SCHED();
  }
  auto r = __builtin_amdgcn_permlane32_swap(f2u(o1[0]), f2u(o1[0]), false, false);
  auto t = __builtin_amdgcn_permlane16_swap(r[1], r[1], false, false);
  const float inv = __builtin_amdgcn_rcpf(u2f(t[1]));
  return u32x4{pk(o0[0] * inv, o0[1] * inv), pk(o0[2] * inv, o0[3] * inv), pk(o1[0] * inv, o1[1] * inv), pk(o1[2] * inv, o1[3] * inv)};
}

__device__ __forceinline__ u32x4 geglu8(const f32x16& acc, f32x2 ms, const f32x4& cg0, const f32x4& cg1, const f32x4& cu0, const f32x4& cu1) {
  const float q = ms.y * ms.y, k1 = GC1 * ms.y, k2 = GC2 * ms.y * q, ir = __builtin_amdgcn_rcpf(q), nm = -ms.x;
  float a[8], b[8], t[8];
#pragma unroll
  for (int r = 0; r < 8; ++r) { a[r] = __builtin_fmaf(nm, r < 4 ? cg0[r & 3] : cg1[r & 3], acc[r]); OPQ(a[r]); }
  SCHED();
#pragma unroll
  for (int r = 0; r < 8; ++r) { t[r] = a[r] * a[r]; OPQ(t[r]); }
  SCHED();
#pragma unroll
  for (int r = 0; r < 8; ++r) { t[r] = __builtin_fmaf(t[r], k2, k1); OPQ(t[r]); }
  SCHED();
#pragma unroll
  for (int r = 0; r < 8; ++r) { t[r] = t[r] * a[r]; OPQ(t[r]); }
  SCHED();
#pragma unroll
  for (int r = 0; r < 8; ++r) {
    t[r] = __builtin_amdgcn_exp2f(t[r]);
    b[r] = __builtin_fmaf(nm, r < 4 ? cu0[r & 3] : cu1[r & 3], acc[r + 8]); OPQ(b[r]);
  }
  SCHED();
#pragma unroll
  for (int r = 0; r < 8; ++r) { t[r] = __builtin_fmaf(t[r], ir, ir); OPQ(t[r]); }
  SCHED();
#pragma unroll
  for (int r = 0; r < 8; ++r) { t[r] = __builtin_amdgcn_rcpf(t[r]); b[r] = a[r] * b[r]; OPQ(b[r]); }
  SCHED();
#pragma unroll
  for (int r = 0; r < 8; ++r) { t[r] = t[r] * b[r]; OPQ(t[r]); }
  SCHED();
  return u32x4{pks(t[0], t[1]), pks(t[2], t[3]), pks(t[4], t[5]), pks(t[6], t[7])};
}

#define IDS()                                                                                                   \
  const int lane = opaque_v(lane0), wave = opaque(wave0), b = opaque(b0), xcd = b & 7, rk = b >> 3;             \
  const int c1 = 4 * (xcd >> 1) + (rk & 3), h1 = 8 * (xcd & 1) + (rk >> 2), c3 = 2 * xcd + (rk & 1), m3 = rk >> 1; \
  const bool act5 = m3 < 14;                                                                                    \
  const int th = m3 >= 7 ? 1 : 0, ng = m3 - 7 * th, i = lane & 15, g = lane >> 4;                               \
  const int part = wave < 6 ? 2 - (wave >> 1) : 0, nt1 = wave & 1, nt6 = 2 * part + nt1, w7 = min(wave, 6), J4 = 7 * h1 + w7, J3 = 7 * (m3 & 3) + w7; \
  const unsigned l16 = lane * 16, half_off = (lane & 31) * 16, res_off = (i + 16 * (g & 1)) * 16 + 8 * (g >> 1), f32_off = (g & 1) * 512 + (i + 16 * (g >> 1)) * 16; \
  (void)c1; (void)h1; (void)c3; (void)act5; (void)ng; (void)part; (void)nt1; (void)J4; (void)J3; (void)l16; (void)half_off; (void)res_off; (void)f32_off; (void)nt6
__global__ __launch_bounds__(NT, 2) void k_main(char* ws, uint16_t* out) {
  extern __shared__ __attribute__((aligned(16))) char sm[];
  const int tid = threadIdx.x, lane0 = tid & 63, wave0 = __builtin_amdgcn_readfirstlane(tid >> 6), b0 = blockIdx.x;
  IDS();
  lflag fl = (lflag)(LAS char*)(sm + L_FL);
  const rsrc_t rs = __builtin_amdgcn_make_buffer_rsrc(ws, 0, WS_USED, 0x00020000);
  int stn = 0; (void)stn;
  if (tid < 64) fl[tid] = 0u;
  __syncthreads();

  const unsigned smb = (unsigned)(uintptr_t)(LAS char*)sm;
  u32x4 w1[14];
  if (wave0 == 7) {
    PRIO(3);
    dma1k(ws, OFF_CS + h1 * 2048 + l16, smb + L_CS);
    dma1k(ws, OFF_CS + h1 * 2048 + 1024 + l16, smb + L_CS + 1024);
  } else {
#pragma unroll
    for (int s = 0; s < 14; ++s) w1[s] = ldwf(rs, l16, OFF_WQKV + (h1 * 6 + nt6) * 14 * KB, s);
  }

#pragma unroll 1
  for (int l = 0; l < 2; ++l) {
    const unsigned st = 5 * l;
    u32x4 w3[16], w4[28], w5[32];
#define PRE1(k)                                                                                         \
  do {                                                                                                  \
    if ((k) < 16) w3[(k)] = ldwf(rs, l16, OFF_WO + J3 * 16 * KB, (k));                                  \
    else w4[(k) - 16] = ldwf(rs, l16, OFF_WGU + J4 * 28 * KB, (k) - 16);                                \
  } while (0)
#define PRE34(k0, k1)                                                                                   \
  SCHED();                                                                                              \
  _Pragma("unroll") for (int k = k0; k < k1; ++k) PRE1(k);                                              \
  SCHED();
    if (wave0 == 7) {
      IDS();
#pragma unroll 1
      for (int X = 0; X < 2; ++X) {
        if (l) {
          if (act5) lwait(fl, F_ARR5 + X, 4); else lwait(fl, F_ARR4 + X, 7);
          gpoll(ws, CNT_X2 + (4 * c1 + 2 * X + (lane & 1)) * 128, 7);
        }
        STAMP();
        serve_x(ws, sm, smb, fl, l ? 2 : 4, 8 * c1 + 4 * X, X, st + 1, lane);
        STAMP();
      }
      lwait(fl, F_ARR1, 6 * (l + 1));
#pragma unroll 1
      for (int k = 0; k < 8 - 4 * c1; ++k) {
        const u32x4 kz = {0u, 0u, g == 3 ? 0xff80u : 0u, 0u}, vz = {0u, 0u, 0u, 0u};
        char* d = sm + L_HALO + k * 4096 + l16;
        *(u32x4*)d = kz; *(u32x4*)(d + 1024) = kz; *(u32x4*)(d + 2048) = vz; *(u32x4*)(d + 3072) = vz;
      }
      if (c1 > 0) gpoll(ws, CNT_KV + ((l * 16 + h1) * 16 + max(c1 - 2 + (lane & 1), 0)) * 128, 4);
      STAMP();
      const unsigned src = OFF_KV + l * KV_BYTES + (h1 * 64 + 4 * c1 - 8) * 4096 + l16;
#pragma unroll
      for (int k = 0; k < 4; ++k) if (2 * c1 + k >= 4) dma8k(ws, src + k * 8192, smb + L_HALO + k * 8192);
      WAIT_VM(0);
      PRIO(0);
    } else {
      touch<14>(w1);
      auto half1 = [&](auto x_t) {
        constexpr int X = decltype(x_t)::value;
        IDS();
        if (wave < 6) {
          lwait(fl, F_RDY + X, st + 1);
          STAMP();
          const char* xs = sm + X * L_REG + (g >> 1) * 1024 + half_off;
          const char* stp = sm + L_ST + X * 512;
          const unsigned so_kv = OFF_KV + l * KV_BYTES + (h1 * 64 + 4 * c1 + 2 * X) * 4096;
          f32x4 acc[4];
          u32x4 xf[14][4];
          unsigned sfl;
#pragma unroll
          for (int t = 0; t < 4; ++t) acc[t] = f32x4{0.f, 0.f, 0.f, 0.f};
#define P1_X(s, t) lds16(xs + ((t) >> 1) * 28 * 1024 + (s) * 2048 + ((t) & 1) * 512)
#define P1_LOOP(MM)                                                                                     \
  _Pragma("unroll") for (int s = 0; s < 2; ++s) {                                                       \
    _Pragma("unroll") for (int t = 0; t < 4; ++t) xf[s][t] = P1_X(s, t);                                \
  }                                                                                                     \
  _Pragma("unroll") for (int s = 0; s < 14; ++s) {                                                      \
    if (s + 2 < 14) { _Pragma("unroll") for (int t = 0; t < 4; ++t) xf[s + 2][t] = P1_X(s + 2, t); }    \
    if (s == 12) { sfl = fl[F_SRDY + X]; asm volatile("" ::: "memory"); P1_EPI_LOADS }                  \
    SCHED();                                                                                            \
    _Pragma("unroll") for (int t = 0; t < 4; ++t) acc[t] = MM;                                          \
    SCHED();                                                                                            \
  }                                                                                                     \
  if ((unsigned)__builtin_amdgcn_readfirstlane(sfl) < st + 1) { lwait(fl, F_SRDY + X, st + 1); P1_EPI_LOADS }
          if (part < 2) {
            f32x4 cs1; f32x2 m[4];
#define P1_EPI_LOADS cs1 = *(const f32x4*)(sm + L_CS + (nt6 * 16 + 4 * g) * 4); _Pragma("unroll") for (int t = 0; t < 4; ++t) m[t] = *(const f32x2*)(stp + t * 128 + i * 8);
            P1_LOOP(mfma(w1[s], xf[s][t], acc[t]))
#undef P1_EPI_LOADS
            STAMP();
            PRIO(2);
            float v[16];
#pragma unroll
            for (int k = 0; k < 16; ++k) { v[k] = __builtin_fmaf(-m[k >> 2].x, cs1[k & 3], acc[k >> 2][k & 3]); OPQ(v[k]); }
            SCHED();
#pragma unroll
            for (int k = 0; k < 16; ++k) { v[k] *= m[k >> 2].y; OPQ(v[k]); }
            SCHED();
            if (nt6 == 1 && g == 3) { v[0] = 1.f; v[4] = 1.f; v[8] = 1.f; v[12] = 1.f; }
            const u32x4 o0 = pair16(u32x2{pk(v[0], v[1]), pk(v[2], v[3])}, u32x2{pk(v[4], v[5]), pk(v[6], v[7])});
            const u32x4 o1 = pair16(u32x2{pk(v[8], v[9]), pk(v[10], v[11])}, u32x2{pk(v[12], v[13]), pk(v[14], v[15])});
            const unsigned off = (g >> 1) * 1024 + nt1 * 512 + half_off;
            if (part == 0) {
              *(u32x4*)(sm + L_Q + X * 4096 + off) = o0;
              *(u32x4*)(sm + L_Q + X * 4096 + 2048 + off) = o1;
            } else {
              sth(o0, ws, off, so_kv); sth(o1, ws, off, so_kv + 4096);
              *(u32x4*)(sm + L_OWN + X * 8192 + off) = o0;
              *(u32x4*)(sm + L_OWN + X * 8192 + 4096 + off) = o1;
            }
          } else {
            float c; f32x4 ma[4], mb[4];
#define P1_EPI_LOADS c = *(const float*)(sm + L_CS + (nt6 * 16 + i) * 4); _Pragma("unroll") for (int t = 0; t < 4; ++t) { ma[t] = *(const f32x4*)(stp + t * 128 + g * 32); mb[t] = *(const f32x4*)(stp + t * 128 + g * 32 + 16); }
            P1_LOOP(mfma(xf[s][t], w1[s], acc[t]))
#undef P1_EPI_LOADS
            STAMP();
            PRIO(2);
            float v[16];
#pragma unroll
            for (int k = 0; k < 16; ++k) { const f32x4 mm = (k & 2) ? mb[k >> 2] : ma[k >> 2]; v[k] = __builtin_fmaf(-mm[2 * (k & 1)], c, acc[k >> 2][k & 3]); OPQ(v[k]); }
            SCHED();
#pragma unroll
            for (int k = 0; k < 16; ++k) { const f32x4 mm = (k & 2) ? mb[k >> 2] : ma[k >> 2]; v[k] *= mm[2 * (k & 1) + 1]; OPQ(v[k]); }
            SCHED();
            u32x4 o0 = {pk(v[0], v[1]), pk(v[2], v[3]), pk(v[4], v[5]), pk(v[6], v[7])};
            u32x4 o1 = {pk(v[8], v[9]), pk(v[10], v[11]), pk(v[12], v[13]), pk(v[14], v[15])};
            if (nt1 == 1 && i == 12) { o0 = u32x4{0x3f803f80u, 0x3f803f80u, 0x3f803f80u, 0x3f803f80u}; o1 = o0; }
            const unsigned off = 2048 + nt1 * 1024 + l16;
            sth(o0, ws, off, so_kv); sth(o1, ws, off, so_kv + 4096);
            *(u32x4*)(sm + L_OWN + X * 8192 + off) = o0;
            *(u32x4*)(sm + L_OWN + X * 8192 + 4096 + off) = o1;
          }
          if (X == 0) arrive_nr(fl, F_ARR1);
          STAMP();
        }
        if (X == 1) {
          PRE34(0, 4)
          if (wave < 4) {
            WAIT_W(4);
            gadd(ws, CNT_KV + ((l * 16 + h1) * 16 + c1) * 128);
            STAMP();
          }
        }
        PRIO(0);
      };
      half1(std::integral_constant<int, 0>{});
      half1(std::integral_constant<int, 1>{});
    }
    BAR();
    {
      IDS();
      STAMP();
      const int T = 8 * c1 + wave;
      const u32x4 q = lds16(sm + L_Q + wave * 1024 + l16);
      const char* kv = sm + L_HALO + (wave >> 1) * 4096 + l16;
      const u32x4 o = attn_tile(kv, q, wave, lane, [&](int k) { PRE1(4 + k); });
      sth(o, ws, l16, OFF_O + l * O_BYTES + (T * 16 + h1) * KB);
      STAMP();
      PRE34(18, 22)
      WAIT_W(4);
      gadd(ws, CNT_O + (l * 128 + T) * 128);
      STAMP();
      if (wave == 7) {
        PRIO(3);
        const int P = 4 * c3 + (m3 >> 2);
        gpoll(ws, CNT_O + (l * 128 + 2 * P + (lane & 1)) * 128, 16);
        STAMP();
        const unsigned src = OFF_O + l * O_BYTES + P * 32 * KB + l16;
#pragma unroll
        for (int k = 0; k < 4; ++k) dma8k(ws, src + k * 8192, smb + L_REG + k * 8192);
        dma8k(ws, OFF_X + 2 * l * X_BYTES + (P * 28 + 7 * (m3 & 3)) * KB + l16, smb + L_RES);
        WAIT_VM(0);
      }
    }
    BAR();
    if (wave0 == 7) {
      IDS();
#pragma unroll 1
      for (int X = 0; X < 2; ++X) {
        lwait(fl, F_ARR3, 7 * (l + 1));
        gpoll(ws, CNT_X1 + (l * 64 + 4 * c1 + 2 * X + (lane & 1)) * 128, 4);
        STAMP();
        serve_x(ws, sm, smb, fl, 2 * l + 1, 8 * c1 + 4 * X, X, st + 4, lane);
        STAMP();
      }
      if (act5) {
#pragma unroll 1
        for (int X = 0; X < 2; ++X) {
          lwait(fl, F_ARR4 + X, 7 * (l + 1));
          gpoll(ws, CNT_H + ((l * 16 + c3) * 2 + X) * 128, 16);
          fl[F_RDY + X] = st + 5;
          STAMP();
          const int P = 4 * c3 + 2 * X + th;
          const unsigned src = OFF_H + l * H_BYTES + P * 112 * KB + f32_off;
#pragma unroll
          for (int k = 0; k < 7; ++k) dma8k(ws, src + k * 16 * KB, smb + X * L_REG + k * 8192);
          dma4k(ws, OFF_X + (2 * l + 1) * X_BYTES + (P * 28 + 4 * ng) * KB + f32_off, smb + L_RES + X * 4096);
          WAIT_VM(0);
          fl[F_SRDY + X] = st + 5;
          STAMP();
        }
      }
      undef<14>(w1);
    } else {
      {
        IDS();
        STAMP();
        const char* os = sm + L_REG + l16;
        u32x4 oa[16], ob[16];
#pragma unroll
        for (int s = 0; s < 6; ++s) { oa[s] = lds16(os + s * 1024); ob[s] = lds16(os + (16 + s) * 1024); }
        const u32x2 xa = *(const u32x2*)(sm + L_RES + wave * 1024 + res_off), xb = *(const u32x2*)(sm + L_RES + wave * 1024 + 512 + res_off);
        f32x4 a0 = {0.f, 0.f, 0.f, 0.f}, a1 = {0.f, 0.f, 0.f, 0.f};
#pragma unroll
        for (int s = 0; s < 16; ++s) {
          SCHED();
          a0 = mfma(w3[s], oa[s], a0); a1 = mfma(w3[s], ob[s], a1);
          SCHED();
          if (s + 6 < 16) { oa[s + 6] = lds16(os + (s + 6) * 1024); ob[s + 6] = lds16(os + (22 + s) * 1024); }
        }
        PRIO(2);
        float v[8] = {a0[0] + bflo(xa.x), a0[1] + bfhi(xa.x), a0[2] + bflo(xa.y), a0[3] + bfhi(xa.y),
                      a1[0] + bflo(xb.x), a1[1] + bfhi(xb.x), a1[2] + bflo(xb.y), a1[3] + bfhi(xb.y)};
#pragma unroll
        for (int k = 0; k < 8; ++k) OPQ(v[k]);
        const int P = 4 * c3 + (m3 >> 2);
        sth(pair16(u32x2{pk(v[0], v[1]), pk(v[2], v[3])}, u32x2{pk(v[4], v[5]), pk(v[6], v[7])}), ws, l16,
            OFF_X + (2 * l + 1) * X_BYTES + (P * 28 + J3) * KB);
        float s1a = v[0] + v[1], s1b = v[4] + v[5], s2a = v[0] * v[0], s2b = v[4] * v[4];
        OPQ(s1a); OPQ(s1b); OPQ(s2a); OPQ(s2b);
        s1a += v[2]; s1b += v[6]; s2a = __builtin_fmaf(v[1], v[1], s2a); s2b = __builtin_fmaf(v[5], v[5], s2b);
        OPQ(s1a); OPQ(s1b); OPQ(s2a); OPQ(s2b);
        s1a += v[3]; s1b += v[7]; s2a = __builtin_fmaf(v[2], v[2], s2a); s2b = __builtin_fmaf(v[6], v[6], s2b);
        OPQ(s2a); OPQ(s2b);
        s2a = __builtin_fmaf(v[3], v[3], s2a); s2b = __builtin_fmaf(v[7], v[7], s2b);
        auto r1 = __builtin_amdgcn_permlane32_swap(f2u(s1a), f2u(s1b), false, false);
        auto r2 = __builtin_amdgcn_permlane32_swap(f2u(s2a), f2u(s2b), false, false);
        float s1 = u2f(r1[0]) + u2f(r1[1]), s2 = u2f(r2[0]) + u2f(r2[1]);
        OPQ(s1); OPQ(s2);
        auto t = __builtin_amdgcn_permlane16_swap(f2u(s1), f2u(s2), false, false);
        sth32(f2u(u2f(t[0]) + u2f(t[1])), ws, (g >> 1) * 3584 + (lane & 31) * 4, OFF_ST + (2 * l + 1) * ST_BYTES + (2 * P * 28 + J3) * 128);
        STAMP();
        PRE34(22, 28)
        WAIT_W(6);
        if (arrive(fl, F_ARR3, 7 * (l + 1))) gadd(ws, CNT_X1 + (l * 64 + P) * 128);
        PRIO(0);
        STAMP();
      }
      auto half4 = [&](auto x_t) {
        constexpr int X = decltype(x_t)::value;
        IDS();
        lwait(fl, F_RDY + X, st + 4);
        STAMP();
        PRIO(1 - X);
        const char* xs = sm + X * L_REG + f32_off;
        const char* stp = sm + L_ST + X * 512 + (lane & 31) * 8;
        const char* csp = sm + L_CS + (96 + wave * 16 + 4 * (lane >> 5)) * 4;
        f32x16 a0, a1;
#pragma unroll
        for (int r = 0; r < 16; ++r) { a0[r] = 0.f; a1[r] = 0.f; }
        u32x4 xa[28], xb[28];
        f32x2 m0, m1; f32x4 cg0, cg1, cu0, cu1;
        unsigned sfl;
#define P4_LOOP(STREAM)                                                                                 \
  _Pragma("unroll") for (int s = 0; s < 2; ++s) { xa[s] = lds16(xs + s * 1024); xb[s] = lds16(xs + (28 + s) * 1024); } \
  _Pragma("unroll") for (int s = 0; s < 28; ++s) {                                                      \
    if (s + 2 < 28) { xa[s + 2] = lds16(xs + (s + 2) * 1024); xb[s + 2] = lds16(xs + (30 + s) * 1024); } \
    if (s == 26) { sfl = fl[F_SRDY + X]; asm volatile("" ::: "memory"); m0 = *(const f32x2*)stp; m1 = *(const f32x2*)(stp + 256); \
                   cg0 = *(const f32x4*)csp; cg1 = *(const f32x4*)(csp + 32); }                         \
    if (s == 27) { cu0 = *(const f32x4*)(csp + 448); cu1 = *(const f32x4*)(csp + 480); }                \
    if (STREAM && s < 8) { PRE1(28 + 2 * s); PRE1(29 + 2 * s); }                                        \
    SCHED();                                                                                            \
    a0 = mfma32(w4[s], xa[s], a0); a1 = mfma32(w4[s], xb[s], a1);                                       \
    SCHED();                                                                                            \
  }
        if (X == 0) {
          if (wave < 4) { P4_LOOP(true) }
          else { PRE34(28, 44) P4_LOOP(false) }
        } else {
          P4_LOOP(false)
        }
        if ((unsigned)__builtin_amdgcn_readfirstlane(sfl) < st + 4) {
          lwait(fl, F_SRDY + X, st + 4);
          m0 = *(const f32x2*)stp; m1 = *(const f32x2*)(stp + 256);
        }
        STAMP();
        PRIO(2);
        const unsigned so = OFF_H + l * H_BYTES + ((4 * c1 + 2 * X) * 112 + J4) * KB;
        sth(geglu8(a0, m0, cg0, cg1, cu0, cu1), ws, f32_off, so);
        sth(geglu8(a1, m1, cg0, cg1, cu0, cu1), ws, f32_off, so + 112 * KB);
        STAMP();
        SCHED();
        const unsigned so5 = OFF_WD + ((act5 ? ng : 0) * 112 + 16 * wave) * 2 * KB;
#pragma unroll
        for (int k = 0; k < 16; ++k) w5[16 * X + k] = ldwf(rs, l16, so5, 16 * X + k);
        SCHED();
        WAIT_W(16);
        if (arrive(fl, F_ARR4 + X, 7 * (l + 1))) gadd(ws, CNT_H + ((l * 16 + c1) * 2 + X) * 128);
        PRIO(0);
        STAMP();
      };
      half4(std::integral_constant<int, 0>{});
      half4(std::integral_constant<int, 1>{});
      SCHED();
      u32x4 hf[8];
      auto load5 = [&](int X) {
        IDS();
        const unsigned so = OFF_H + l * H_BYTES + ((4 * c3 + 2 * X + th) * 112 + 16 * wave + 8) * KB;
#pragma unroll
        for (int s = 0; s < 8; ++s) hf[s] = ldhf(rs, f32_off, so, s);
      };
      auto mma5 = [&](int X) {
        IDS();
        lwait(fl, F_SRDY + X, st + 5);
        char* mine = sm + X * L_REG + wave * 8192 + l16;
        u32x4 hl[8];
#pragma unroll
        for (int s = 0; s < 8; ++s) hl[s] = lds16(mine + s * 1024);
        f32x16 a0, a1;
#pragma unroll
        for (int r = 0; r < 16; ++r) { a0[r] = 0.f; a1[r] = 0.f; }
#pragma unroll
        for (int s = 0; s < 16; ++s) {
          SCHED();
          a0 = mfma32(w5[2 * s], s < 8 ? hl[s] : hf[s - 8], a0); a1 = mfma32(w5[2 * s + 1], s < 8 ? hl[s] : hf[s - 8], a1);
        }
        SCHED();
#pragma unroll
        for (int k = 0; k < 4; ++k) {
          *(f32x4*)(mine + k * 1024) = f32x4{a0[4 * k], a0[4 * k + 1], a0[4 * k + 2], a0[4 * k + 3]};
          *(f32x4*)(mine + (4 + k) * 1024) = f32x4{a1[4 * k], a1[4 * k + 1], a1[4 * k + 2], a1[4 * k + 3]};
        }
        arrive_nr(fl, F_RED + X);
      };
      auto fin5 = [&](int X) {
        IDS();
        PRIO(2);
        const u32x4 xr = lds16(sm + L_RES + X * 4096 + wave * 1024 + l16);
        lwait(fl, F_RED + X, 7 * (l + 1));
        const char* red = sm + X * L_REG + wave * 2048 + l16;
        f32x4 pa[7], pb[7];
#pragma unroll
        for (int k = 0; k < 7; ++k) { pa[k] = *(const f32x4*)(red + k * 8192); pb[k] = *(const f32x4*)(red + k * 8192 + 1024); }
        float v[8] = {bflo(xr.x), bfhi(xr.x), bflo(xr.y), bfhi(xr.y), bflo(xr.z), bfhi(xr.z), bflo(xr.w), bfhi(xr.w)};
        SCHED();
#pragma unroll
        for (int k = 0; k < 7; ++k) {
#pragma unroll
          for (int r = 0; r < 8; ++r) { v[r] += r < 4 ? pa[k][r] : pb[k][r - 4]; OPQ(v[r]); }
          SCHED();
        }
        const u32x4 o = {pks(v[0], v[1]), pks(v[2], v[3]), pks(v[4], v[5]), pks(v[6], v[7])};
        const int P = 4 * c3 + 2 * X + th, J = 4 * ng + wave;
        if (l == 0) {
          sth(o, ws, f32_off, OFF_X + 2 * X_BYTES + (P * 28 + J) * KB);
          float s1 = v[0] + v[1], s2 = v[0] * v[0], s3 = v[2] + v[3], s4 = v[1] * v[1];
          OPQ(s1); OPQ(s2); OPQ(s3); OPQ(s4);
          s1 += v[4]; s3 += v[5]; s2 = __builtin_fmaf(v[2], v[2], s2); s4 = __builtin_fmaf(v[3], v[3], s4);
          OPQ(s1); OPQ(s2); OPQ(s3); OPQ(s4);
          s1 += v[6]; s3 += v[7]; s2 = __builtin_fmaf(v[4], v[4], s2); s4 = __builtin_fmaf(v[5], v[5], s4);
          OPQ(s1); OPQ(s2); OPQ(s3); OPQ(s4);
          s1 += s3; s2 = __builtin_fmaf(v[6], v[6], s2); s4 = __builtin_fmaf(v[7], v[7], s4);
          OPQ(s1); OPQ(s2); OPQ(s4);
          s2 += s4;
          auto r = __builtin_amdgcn_permlane32_swap(f2u(s1), f2u(s2), false, false);
          const float sv = u2f(r[0]) + u2f(r[1]);
          sth32(f2u(sv), ws, (g & 1) * 3584 + (lane >> 5) * 64 + i * 4, OFF_ST + 2 * ST_BYTES + (2 * P * 28 + J) * 128);
        } else {
          uint16_t* op = out + (size_t)(P * 32 + (lane & 31)) * D_ + 16 * J + 4 * (lane >> 5);
          *(u32x2*)op = u32x2{o.x, o.y};
          *(u32x2*)(op + 8) = u32x2{o.z, o.w};
        }
      };
      auto sig5 = [&](int X) {
        IDS();
        if (arrive(fl, F_ARR5 + X, 4 * (l + 1))) { if (l == 0) gadd(ws, CNT_X2 + (4 * c3 + 2 * X + th) * 128); }
        PRIO(0);
      };
      if (act5) {
        lwait(fl, F_RDY + 0, st + 5);
        STAMP();
        load5(0);
        SCHED();
        mma5(0);
        STAMP();
        bool early = true;
        if (wave0 < 4) {
          fin5(0);
          STAMP();
          early = lready(fl, F_RDY + 1, st + 5);
          if (!early) { WAIT_ALL(); sig5(0); }
        }
        lwait(fl, F_RDY + 1, st + 5);
        STAMP();
        SCHED();
        load5(1);
        SCHED();
        if (wave0 < 4 && early) { asm volatile("s_waitcnt vmcnt(8) lgkmcnt(0)" ::: "memory"); sig5(0); }
        SCHED();
        mma5(1);
        STAMP();
        if (wave0 < 4) fin5(1);
        STAMP();
      }
      SCHED();
      {
        IDS();
#pragma unroll
        for (int s = 0; s < 14; ++s) w1[s] = ldwf(rs, l16, OFF_WQKV + (h1 * 6 + nt6) * 14 * KB, s);
      }
      SCHED();
      if (act5 && wave0 < 4) {
        WAIT_W(14);
        sig5(1);
        STAMP();
      }
    }
  }
}

extern "C" void kernel_launch(void* const* d_in, const int* in_sizes, int n_in, void* d_out, int out_size, void* d_ws,
                              size_t ws_size, hipStream_t stream) {
  const uint16_t* x = (const uint16_t*)d_in[0];
  const uint16_t* wqkv = (const uint16_t*)d_in[1];
  const uint16_t* wo = (const uint16_t*)d_in[2];
  const uint16_t* wg = (const uint16_t*)d_in[4];
  const uint16_t* wu = (const uint16_t*)d_in[5];
  const uint16_t* wd = (const uint16_t*)d_in[6];
  char* ws = (char*)d_ws;
  (void)hipFuncSetAttribute((const void*)k_main, hipFuncAttributeMaxDynamicSharedMemorySize, L_BYTES);
  k_prep<<<256, NT, 0, stream>>>(x, wqkv, wo, wg, wu, wd, ws);
  k_main<<<256, NT, L_BYTES, stream>>>(ws, (uint16_t*)d_out);
}
```

```cpp
#include <hip/hip_runtime.h>
#include <hip/hip_bf16.h>
#include <stdint.h>
#include <type_traits>

typedef short bf16x8 __attribute__((ext_vector_type(8)));
typedef float f32x4 __attribute__((ext_vector_type(4)));
typedef float f32x2 __attribute__((ext_vector_type(2)));
typedef float f32x16 __attribute__((ext_vector_type(16)));
typedef unsigned u32x4 __attribute__((ext_vector_type(4)));
typedef unsigned u32x2 __attribute__((ext_vector_type(2)));
typedef __bf16 bf16x2_t __attribute__((ext_vector_type(2)));
#define GAS __attribute__((address_space(1)))
#define LAS __attribute__((address_space(3)))
typedef GAS unsigned gu32;
typedef volatile LAS unsigned* lflag;
#define RLX_AGENT __ATOMIC_RELAXED, __HIP_MEMORY_SCOPE_AGENT

constexpr int D_ = 448, DH = 28, F_ = 1792;
constexpr int NT = 512;
constexpr unsigned KB = 1024;

constexpr unsigned CNT_X2 = 0;
constexpr unsigned CNT_O  = CNT_X2 + 64 * 128;
constexpr unsigned CNT_X1 = CNT_O + 256 * 128;
constexpr unsigned CNT_H  = CNT_X1 + 128 * 128;
constexpr unsigned CNT_KV = CNT_H + 64 * 128;
constexpr unsigned NCTRL  = CNT_KV / 128 + 512;
static_assert(NCTRL == 1024, "k_prep clears four lines per workgroup");
constexpr unsigned OFF_WQKV = 128 * KB;
constexpr unsigned OFF_WO   = OFF_WQKV + 16 * 6 * 14 * KB;
constexpr unsigned OFF_WGU  = OFF_WO + 28 * 16 * KB;
constexpr unsigned OFF_WD   = OFF_WGU + 112 * 28 * KB;
constexpr unsigned OFF_CS   = OFF_WD + 7 * 56 * 4 * KB;
constexpr unsigned X_BYTES  = 128 * 28 * 512;
constexpr unsigned OFF_X    = OFF_CS + 32 * KB;
constexpr unsigned ST_BYTES = 128 * 28 * 128;
constexpr unsigned OFF_ST   = OFF_X + 5 * X_BYTES;
constexpr unsigned KV_BYTES = 16 * 64 * 4 * KB;
constexpr unsigned OFF_KV   = OFF_ST + 4 * ST_BYTES;
constexpr unsigned O_BYTES  = 128 * 16 * KB;
constexpr unsigned OFF_O    = OFF_KV + 2 * KV_BYTES;
constexpr unsigned H_BYTES  = 128 * 56 * KB;
constexpr unsigned OFF_H    = OFF_O + 2 * O_BYTES;
constexpr unsigned WS_USED  = OFF_H + 2 * H_BYTES;
constexpr unsigned OFF_STAMP = 64u << 20;

constexpr int L_REG = 80 * 1024;
constexpr int L_HALO = 24 * 1024, L_OWN = 56 * 1024, L_Q = 72 * 1024;
constexpr int L_RES = 136 * 1024;
constexpr int L_ST = L_RES + 8192;
constexpr int L_CS = L_ST + 1024;
constexpr int L_FL = L_CS + 2048;
constexpr int L_STAMP = L_FL + 256;
constexpr int L_BYTES = L_STAMP + 8 * 128 * 4;
enum { F_RDY = 0, F_SRDY = 2, F_ARR1 = 4, F_ARR3 = 5, F_ARR4 = 6, F_ARR5 = 8, F_RED = 10, F_ABORT = 12, F_GO2 = 13, F_GO3 = 14, F_GO4 = 15, F_GO5 = 16 };

__device__ __forceinline__ f32x4 mfma(u32x4 a, u32x4 b, f32x4 c) {
  return __builtin_amdgcn_mfma_f32_16x16x32_bf16(__builtin_bit_cast(bf16x8, a), __builtin_bit_cast(bf16x8, b), c, 0, 0, 0);
}
__device__ __forceinline__ f32x16 mfma32(u32x4 a, u32x4 b, f32x16 c) {
  return __builtin_amdgcn_mfma_f32_32x32x16_bf16(__builtin_bit_cast(bf16x8, a), __builtin_bit_cast(bf16x8, b), c, 0, 0, 0);
}
__device__ __forceinline__ unsigned pk(float a, float b) {
  bf16x2_t v = {(__bf16)a, (__bf16)b};
  return __builtin_bit_cast(unsigned, v);
}
__device__ __forceinline__ unsigned pks(float a, float b) {
  unsigned r;
  asm("v_cvt_pk_bf16_f32 %0, %1, %2" : "=v"(r) : "v"(a), "v"(b));
  return r;
}
__device__ __forceinline__ float bflo(unsigned u) { return __builtin_bit_cast(float, u << 16); }
__device__ __forceinline__ float bfhi(unsigned u) { return __builtin_bit_cast(float, u & 0xffff0000u); }
__device__ __forceinline__ unsigned f2u(float f) { return __builtin_bit_cast(unsigned, f); }
__device__ __forceinline__ float u2f(unsigned u) { return __builtin_bit_cast(float, u); }
__device__ __forceinline__ u32x4 lds16(const char* p) { return *(const u32x4*)p; }
#define FW(a, b) (a)
#define MAP1 4
#define MAP3 8
#define POLL_SLEEP "0"
#define SCHED() __builtin_amdgcn_sched_barrier(0)
#define OPQ(x) asm("" : "+v"(x))
#define PRIO(n) __builtin_amdgcn_s_setprio(n)
#define WAIT_VM(n) asm volatile("s_waitcnt vmcnt(" #n ")" ::: "memory")
#define WAIT_ALL() asm volatile("s_waitcnt vmcnt(0) lgkmcnt(0)" ::: "memory")
#define WAIT_LGKM() asm volatile("s_waitcnt lgkmcnt(0)" ::: "memory")
typedef __amdgpu_buffer_rsrc_t rsrc_t;
__device__ __forceinline__ u32x4 ldw(rsrc_t rs, unsigned voff, unsigned soff) { return __builtin_amdgcn_raw_buffer_load_b128(rs, voff, soff, 0); }
__device__ __forceinline__ u32x4 ldh(rsrc_t rs, unsigned voff, unsigned soff) { return __builtin_amdgcn_raw_buffer_load_b128(rs, voff, soff, 16); }
__device__ __forceinline__ u32x4 ldwf(rsrc_t rs, unsigned l16, unsigned base, int k) { return ldw(rs, l16 + (k & 3) * 1024, base + (k >> 2) * 4096); }
#define WCNT(n) #n
#define WAIT_W(n) asm volatile("s_waitcnt vmcnt(" WCNT(n) ") lgkmcnt(0)" ::: "memory")
__device__ __forceinline__ u32x4 ldhf(rsrc_t rs, unsigned l16, unsigned base, int k) { return ldh(rs, l16 + (k & 3) * 1024, base + (k >> 2) * 4096); }
__device__ __forceinline__ unsigned opaque(unsigned v) { asm volatile("" : "+s"(v)); return v; }
template <int N> __device__ __forceinline__ void touch(const u32x4* w) {
#pragma unroll
  for (int k = 0; k < N; ++k) asm volatile("" :: "v"(w[k]));
}
template <int N> __device__ __forceinline__ void undef(u32x4* w) {
#pragma unroll
  for (int k = 0; k < N; ++k) asm volatile("" : "=v"(w[k]));
}
__device__ __forceinline__ int opaque_v(int v) { asm volatile("" : "+v"(v)); return v; }
__device__ __forceinline__ void sth(u32x4 v, char* ws, unsigned voff, unsigned soff) {
  asm volatile("global_store_dwordx4 %0, %1, %2 sc1\n\ts_nop 1" :: "v"(voff + soff), "v"(v), "s"(ws) : "memory");
}
__device__ __forceinline__ void sth32(unsigned v, char* ws, unsigned voff, unsigned soff) {
  asm volatile("global_store_dword %0, %1, %2 sc1" :: "v"(voff + soff), "v"(v), "s"(ws) : "memory");
}

#define STAMP() do {} while (0)

typedef short s16x4 __attribute__((ext_vector_type(4)));
__device__ __forceinline__ u32x2 ldtr(const char* p) {
  return __builtin_bit_cast(u32x2, __builtin_amdgcn_ds_read_tr16_b64_v4i16((LAS s16x4*)(LAS char*)p));
}
__device__ __forceinline__ float rbf(float v) { return bflo(pk(v, v) & 0xffffu); }
__device__ __forceinline__ float sum8(u32x4 v) {
  return ((bflo(v.x) + bfhi(v.x)) + (bflo(v.y) + bfhi(v.y))) + ((bflo(v.z) + bfhi(v.z)) + (bflo(v.w) + bfhi(v.w)));
}
__device__ __forceinline__ float dpp_add(float v, const int ctrl) {
  switch (ctrl) {
    case 0: return v + u2f(__builtin_amdgcn_update_dpp(0u, f2u(v), 0xB1, 0xf, 0xf, false));
    case 1: return v + u2f(__builtin_amdgcn_update_dpp(0u, f2u(v), 0x4E, 0xf, 0xf, false));
    case 2: return v + u2f(__builtin_amdgcn_update_dpp(0u, f2u(v), 0x124, 0xf, 0xf, false));
    default: return v + u2f(__builtin_amdgcn_update_dpp(0u, f2u(v), 0x128, 0xf, 0xf, false));
  }
}
__device__ __forceinline__ float sum32(float v) { auto r = __builtin_amdgcn_permlane32_swap(f2u(v), f2u(v), false, false); return u2f(r[0]) + u2f(r[1]); }
__device__ __forceinline__ float sum16(float v) { auto r = __builtin_amdgcn_permlane16_swap(f2u(v), f2u(v), false, false); return u2f(r[0]) + u2f(r[1]); }
__device__ __forceinline__ float wave_sum(float v) { return sum32(sum16(dpp_add(dpp_add(dpp_add(dpp_add(v, 0), 1), 2), 3))); }

#define PST16(v, off) sth((v), ws, (off), 0u)
constexpr int PW_STRIDE = 80, PW_BYTES = 452 * PW_STRIDE, PX_OFF = PW_BYTES, PR_OFF = PX_OFF + 2 * 7168;
__global__ __launch_bounds__(NT) void k_prep(const uint16_t* __restrict__ x, const uint16_t* __restrict__ wqkv,
                                             const uint16_t* __restrict__ wo, const uint16_t* __restrict__ wg,
                                             const uint16_t* __restrict__ wu, const uint16_t* __restrict__ wd, char* ws) {
  __shared__ __attribute__((aligned(16))) char sm[PR_OFF + 8 * 32 * 4];
  const int b = blockIdx.x, tid = threadIdx.x, lane = tid & 63, wave = __builtin_amdgcn_readfirstlane(tid >> 6);
  if (tid < 4) *(unsigned*)(ws + (b * 4 + tid) * 128) = 0u;
  const u32x4 xv = *(const u32x4*)(x + (size_t)(b * 8 + wave) * D_ + min(lane, 55) * 8);
  const uint16_t* src = wo; int ld = D_, ncols = 32, type = 3; char* dst = ws; int csf = -1, csb = 0, s_stride = 0, nt_stride = 0;
  if (b < 48) {
    int h = b / 3, p = b % 3; type = 0;
    src = wqkv + p * D_ + DH * h; ld = 3 * D_; ncols = DH;
    dst = ws + OFF_WQKV + (size_t)(h * 3 + p) * 28 * KB; s_stride = 1024; nt_stride = 14 * 1024;
    csf = h * 112 + p * 32;
  } else if (b < 62) {
    int j = b - 48; type = 1;
    src = wo + 32 * j;
    dst = ws + OFF_WO + (size_t)(2 * j) * 16 * KB; s_stride = 1024; nt_stride = 16 * 1024;
  } else if (b < 174) {
    int j = (b - 62) >> 1, u = (b - 62) & 1; type = 2;
    src = (u ? wu : wg) + 32 * j; ld = F_;
    dst = ws + OFF_WGU + (size_t)(2 * j) * 28 * KB + u * 256; s_stride = 1024; nt_stride = 28 * 1024;
    csf = 32 * j; csb = 96 + 112 * u;
  } else if (b < 230) {
    int j = (b - 174) >> 2, q = (b - 174) & 3; type = 2;
    src = wd + (size_t)q * 448 * D_ + 32 * j;
    dst = ws + OFF_WD + ((size_t)(j >> 1) * 112 + q * 28) * 2 * KB + (j & 1) * KB; s_stride = 2048; nt_stride = 256;
  }
  u32x2 wv[7];
  const int c4 = tid & 7;
  const bool cok = c4 * 4 < ncols;
#pragma unroll
  for (int u = 0; u < 7; ++u) wv[u] = *(const u32x2*)(src + (size_t)(u * 64 + (tid >> 3)) * ld + (cok ? c4 * 4 : 0));
  {
    const float e[8] = {bflo(xv.x), bfhi(xv.x), bflo(xv.y), bfhi(xv.y), bflo(xv.z), bfhi(xv.z), bflo(xv.w), bfhi(xv.w)};
    const float mean = wave_sum(lane < 56 ? sum8(xv) : 0.f) * (1.f / D_);
    float q = 0.f;
#pragma unroll
    for (int k = 0; k < 8; ++k) q += (e[k] - mean) * (e[k] - mean);
    const float var = wave_sum(lane < 56 ? q : 0.f) * (1.f / D_);
    const float mb = rbf(mean), sd = rbf(__builtin_sqrtf(rbf(rbf(var) + 1e-5f)));
    const float isd = 1.f / sd;
    float y[8];
#pragma unroll
    for (int k = 0; k < 8; ++k) y[k] = rbf(e[k] - mb) * isd;
    if (lane < 56) {
      char* d = sm + PX_OFF + (lane >> 1) * 256 + wave * 16 + (lane & 1) * 8;
      *(u32x2*)d = u32x2{xv.x, xv.y}; *(u32x2*)(d + 128) = u32x2{xv.z, xv.w};
      *(u32x2*)(d + 7168) = u32x2{pk(y[0], y[1]), pk(y[2], y[3])}; *(u32x2*)(d + 7168 + 128) = u32x2{pk(y[4], y[5]), pk(y[6], y[7])};
    }
  }
#pragma unroll
  for (int u = 0; u < 7; ++u) *(u32x2*)(sm + (u * 64 + (tid >> 3)) * PW_STRIDE + c4 * 8) = cok ? wv[u] : u32x2{0u, 0u};
  if (tid < 40) *(u32x2*)(sm + 448 * PW_STRIDE + tid * 8) = u32x2{0u, 0u};
  __syncthreads();
  if (tid < 448) {
    const unsigned o = ((b >> 2) * 28 + (tid >> 4)) * KB + ((b >> 1) & 1) * 512 + ((tid >> 3) & 1) * 256 + (b & 1) * 128 + (tid & 7) * 16;
    PST16(lds16(sm + PX_OFF + tid * 16), OFF_X + o);
    PST16(lds16(sm + PX_OFF + 7168 + tid * 16), OFF_X + 4 * X_BYTES + o);
  }
  if (type == 3) return;
  const int i = lane & 15, g = lane >> 4, q4 = i >> 2, p4 = i & 3;
  float csum = 0.f;
#pragma unroll
  for (int k = 0; k < 4; ++k) {
    const int f = wave + 8 * k;
    if (f < (type == 1 ? 32 : 28)) {
      int s, nt, r0, r1;
      if (type == 2) { s = f; nt = g & 1; r0 = 16 * s + 4 * (g >> 1); r1 = r0 + 8; }
      else if (type == 0) { s = f >> 1; nt = f & 1; r0 = 32 * s + 16 * (g >> 1) + 4 * (g & 1); r1 = r0 + 8; }
      else { s = f >> 1; nt = f & 1; r0 = DH * s + 4 * g; r1 = g < 3 ? r0 + 16 : 448; }
      const u32x2 lo = ldtr(sm + (r0 + q4) * PW_STRIDE + nt * 32 + p4 * 8), hi = ldtr(sm + (r1 + q4) * PW_STRIDE + nt * 32 + p4 * 8);
      const u32x4 o = {lo.x, lo.y, hi.x, hi.y};
      PST16(o, (unsigned)(dst - ws) + s * s_stride + nt * nt_stride + (type == 2 ? i * 16 + (g >> 1) * 512 : lane * 16));
      csum += sum8(o);
    }
  }
  if (csf >= 0) {
    csum = sum32(csum);
    if (type == 0) csum = sum16(csum);
    float* red = (float*)(sm + PR_OFF);
    if (lane < 32) red[wave * 32 + lane] = (type == 0 && (lane >> 4) != (wave & 1)) ? 0.f : csum;
    __syncthreads();
    if (tid < 32) {
      float t = 0.f;
#pragma unroll
      for (int w = 0; w < 8; ++w) t += red[w * 32 + tid];
      const int f = csf + tid;
      ((float*)(ws + OFF_CS))[(f / 112) * 512 + csb + f % 112] = t;
    }
  }
}

__device__ __forceinline__ void lwait(lflag fl, int idx, unsigned tgt) {
  unsigned v, sv, n = 1u << 16;
  const unsigned addr = (unsigned)(uintptr_t)(fl + idx);
  asm volatile("1:\n\tds_read_b32 %0, %3\n\ts_waitcnt lgkmcnt(0)\n\tv_readfirstlane_b32 %1, %0\n\ts_cmp_ge_u32 %1, %4\n\t"
               "s_cbranch_scc1 2f\n\ts_sleep 1\n\ts_sub_u32 %2, %2, 1\n\ts_cbranch_scc0 1b\n2:"
               : "=&v"(v), "=&s"(sv), "+s"(n) : "v"(addr), "s"(tgt) : "memory", "scc");
}
__device__ __forceinline__ bool lready(lflag fl, int idx, unsigned tgt) {
  bool r = (unsigned)__builtin_amdgcn_readfirstlane(fl[idx]) >= tgt;
  asm volatile("" ::: "memory");
  return r;
}
__device__ __forceinline__ bool arrive(lflag fl, int idx, unsigned tgt) {
  unsigned old, so;
  const unsigned addr = (unsigned)(uintptr_t)(fl + idx), one = 1u;
  asm volatile("s_mov_b64 exec, 1\n\tds_add_rtn_u32 %0, %2, %3\n\ts_mov_b64 exec, -1\n\ts_waitcnt lgkmcnt(0)\n\tv_readfirstlane_b32 %1, %0"
               : "=&v"(old), "=s"(so) : "v"(addr), "v"(one) : "memory");
  return so + 1u == tgt;
}
__device__ __forceinline__ void arrive_nr(lflag fl, int idx) {
  const unsigned addr = (unsigned)(uintptr_t)(fl + idx), one = 1u;
  asm volatile("s_mov_b64 exec, 1\n\tds_add_u32 %0, %1\n\ts_mov_b64 exec, -1" :: "v"(addr), "v"(one) : "memory");
}
__device__ __forceinline__ void gadd(char* ws, unsigned off) {
  const unsigned one = 1u;
  asm volatile("s_mov_b64 exec, 1\n\tglobal_atomic_add %0, %1, %2\n\ts_mov_b64 exec, -1" :: "v"(off), "v"(one), "s"(ws) : "memory");
}
__device__ __forceinline__ void gpoll(char* ws, unsigned off0, unsigned off1, unsigned tgt) {
  unsigned a, c, n = 1u << 15;
  off0 = __builtin_amdgcn_readfirstlane(off0); off1 = __builtin_amdgcn_readfirstlane(off1);
  asm volatile("s_nop 4\n1:\n\ts_load_dword %0, %3, %4 glc\n\ts_load_dword %1, %3, %5 glc\n\ts_waitcnt lgkmcnt(0)\n\ts_min_u32 %0, %0, %1\n\t"
               "s_cmp_ge_u32 %0, %6\n\ts_cbranch_scc1 2f\n\ts_sleep " POLL_SLEEP "\n\ts_sub_u32 %2, %2, 1\n\ts_cbranch_scc0 1b\n2:"
               : "=&s"(a), "=&s"(c), "+s"(n) : "s"(ws), "s"(off0), "s"(off1), "s"(tgt) : "memory", "scc");
}
__device__ __forceinline__ bool gready(char* ws, unsigned off, unsigned tgt) {
  unsigned a;
  off = __builtin_amdgcn_readfirstlane(off);
  asm volatile("s_nop 4\n\ts_load_dword %0, %1, %2 glc\n\ts_waitcnt lgkmcnt(0)" : "=&s"(a) : "s"(ws), "s"(off) : "memory");
  return a >= tgt;
}
__device__ __forceinline__ void warm(const char* ws, unsigned off, int nlines, int lane) {
  unsigned v, a[7];
#pragma unroll
  for (int k = 0; k < 7; ++k) a[k] = off + min(k * 64 + lane, nlines - 1) * 128;
  asm volatile("global_load_dword %0, %1, %8 sc1\n\tglobal_load_dword %0, %2, %8 sc1\n\tglobal_load_dword %0, %3, %8 sc1\n\t"
               "global_load_dword %0, %4, %8 sc1\n\tglobal_load_dword %0, %5, %8 sc1\n\tglobal_load_dword %0, %6, %8 sc1\n\t"
               "global_load_dword %0, %7, %8 sc1\n\ts_waitcnt vmcnt(0)"
               : "=&v"(v) : "v"(a[0]), "v"(a[1]), "v"(a[2]), "v"(a[3]), "v"(a[4]), "v"(a[5]), "v"(a[6]), "s"(ws) : "memory");
}
#define BAR() do { asm volatile("s_waitcnt lgkmcnt(0)" ::: "memory"); __builtin_amdgcn_s_barrier(); asm volatile("" ::: "memory"); } while (0)
__device__ __forceinline__ void dma8k(const char* ws, unsigned voff, unsigned lds) {
  unsigned keep;
  lds = __builtin_amdgcn_readfirstlane(lds + 4096);
  asm volatile("s_mov_b32 %0, m0\n\ts_mov_b32 m0, %3\n\ts_nop 0\n\t"
               "global_load_lds_dwordx4 %1, %2 offset:-4096 sc1\n\tglobal_load_lds_dwordx4 %1, %2 offset:-3072 sc1\n\t"
               "global_load_lds_dwordx4 %1, %2 offset:-2048 sc1\n\tglobal_load_lds_dwordx4 %1, %2 offset:-1024 sc1\n\t"
               "global_load_lds_dwordx4 %1, %2 sc1\n\tglobal_load_lds_dwordx4 %1, %2 offset:1024 sc1\n\t"
               "global_load_lds_dwordx4 %1, %2 offset:2048 sc1\n\tglobal_load_lds_dwordx4 %1, %2 offset:3072 sc1\n\t"
               "s_mov_b32 m0, %0"
               : "=&s"(keep) : "v"(voff + 4096), "s"(ws), "s"(lds) : "memory");
}
__device__ __forceinline__ void dma4k(const char* ws, unsigned voff, unsigned lds) {
  unsigned keep;
  lds = __builtin_amdgcn_readfirstlane(lds);
  asm volatile("s_mov_b32 %0, m0\n\ts_mov_b32 m0, %3\n\ts_nop 0\n\t"
               "global_load_lds_dwordx4 %1, %2 sc1\n\tglobal_load_lds_dwordx4 %1, %2 offset:1024 sc1\n\t"
               "global_load_lds_dwordx4 %1, %2 offset:2048 sc1\n\tglobal_load_lds_dwordx4 %1, %2 offset:3072 sc1\n\t"
               "s_mov_b32 m0, %0"
               : "=&s"(keep) : "v"(voff), "s"(ws), "s"(lds) : "memory");
}
__device__ __forceinline__ void dma1k(const char* ws, unsigned voff, unsigned lds) {
  unsigned keep;
  lds = __builtin_amdgcn_readfirstlane(lds);
  asm volatile("s_mov_b32 %0, m0\n\ts_mov_b32 m0, %3\n\ts_nop 0\n\tglobal_load_lds_dwordx4 %1, %2 sc1\n\ts_mov_b32 m0, %0"
               : "=&s"(keep) : "v"(voff), "s"(ws), "s"(lds) : "memory");
}

constexpr float GC1 = -2.f * 0.7978845608028654f * 1.4426950408889634f, GC2 = GC1 * 0.044715f;
__device__ __forceinline__ u32x4 pair16(u32x2 a, u32x2 b) {
  auto rx = __builtin_amdgcn_permlane32_swap(a.x, b.x, false, false);
  auto ry = __builtin_amdgcn_permlane32_swap(a.y, b.y, false, false);
  return u32x4{rx[0], ry[0], rx[1], ry[1]};
}
__device__ __forceinline__ float allg_max(float m) {
  auto r = __builtin_amdgcn_permlane32_swap(f2u(m), f2u(m), false, false);
  m = fmaxf(u2f(r[0]), u2f(r[1]));
  auto q = __builtin_amdgcn_permlane16_swap(f2u(m), f2u(m), false, false);
  return fmaxf(u2f(q[0]), u2f(q[1]));
}

__device__ __forceinline__ void dma_x(char* ws, unsigned smb, int xi, int T0, int X, int lane) {
  const unsigned src = OFF_X + xi * X_BYTES + (unsigned)T0 * 14 * KB + lane * 16;
#pragma unroll
  for (int k = 0; k < 7; ++k) dma8k(ws, src + k * 8192, smb + X * L_REG + k * 8192);
}
__device__ __forceinline__ void stat_loads(char* ws, u32x4 (&p)[14], int xi, int T0, int lane) {
  const unsigned vo = (lane >> 4) * 3584 + (lane & 15) * 16 + OFF_ST + xi * ST_BYTES + (unsigned)T0 * 3584;
  asm volatile("global_load_dwordx4 %0, %14, %15 sc1\n\tglobal_load_dwordx4 %1, %14, %15 offset:256 sc1\n\t"
               "global_load_dwordx4 %2, %14, %15 offset:512 sc1\n\tglobal_load_dwordx4 %3, %14, %15 offset:768 sc1\n\t"
               "global_load_dwordx4 %4, %14, %15 offset:1024 sc1\n\tglobal_load_dwordx4 %5, %14, %15 offset:1280 sc1\n\t"
               "global_load_dwordx4 %6, %14, %15 offset:1536 sc1\n\tglobal_load_dwordx4 %7, %14, %15 offset:1792 sc1\n\t"
               "global_load_dwordx4 %8, %14, %15 offset:2048 sc1\n\tglobal_load_dwordx4 %9, %14, %15 offset:2304 sc1\n\t"
               "global_load_dwordx4 %10, %14, %15 offset:2560 sc1\n\tglobal_load_dwordx4 %11, %14, %15 offset:2816 sc1\n\t"
               "global_load_dwordx4 %12, %14, %15 offset:3072 sc1\n\tglobal_load_dwordx4 %13, %14, %15 offset:3328 sc1\n\t"
               "s_waitcnt vmcnt(14)"
               : "=&v"(p[0]), "=&v"(p[1]), "=&v"(p[2]), "=&v"(p[3]), "=&v"(p[4]), "=&v"(p[5]), "=&v"(p[6]), "=&v"(p[7]), "=&v"(p[8]),
                 "=&v"(p[9]), "=&v"(p[10]), "=&v"(p[11]), "=&v"(p[12]), "=&v"(p[13])
               : "v"(vo), "s"(ws) : "memory");
}
__device__ __forceinline__ bool stat_wait_poll(char* ws, u32x4 (&p)[14], unsigned off, unsigned tgt) {
  unsigned v;
  asm volatile("global_load_dword %0, %15, %16 sc1\n\ts_waitcnt vmcnt(0)"
               : "=&v"(v), "+v"(p[0]), "+v"(p[1]), "+v"(p[2]), "+v"(p[3]), "+v"(p[4]), "+v"(p[5]), "+v"(p[6]), "+v"(p[7]), "+v"(p[8]), "+v"(p[9]),
                 "+v"(p[10]), "+v"(p[11]), "+v"(p[12]), "+v"(p[13]) : "v"(off), "s"(ws) : "memory");
  return __all(v >= tgt);
}
__device__ __forceinline__ void stat_finish(char* sm, lflag fl, const u32x4 (&p)[14], int X, unsigned stage, int lane) {
  float a[4];
#pragma unroll
  for (int r = 0; r < 4; ++r) {
    float t0 = u2f(p[0][r]) + u2f(p[1][r]), t1 = u2f(p[2][r]) + u2f(p[3][r]);
    OPQ(t0); OPQ(t1);
#pragma unroll
    for (int k = 4; k < 14; k += 2) { t0 += u2f(p[k][r]); t1 += u2f(p[k + 1][r]); OPQ(t0); OPQ(t1); }
    a[r] = t0 + t1;
  }
  float ms[4], rsd[4];
#pragma unroll
  for (int r = 0; r < 4; ++r) {
    float s1 = a[r] + u2f(__builtin_amdgcn_update_dpp(0u, f2u(a[r]), 0x128, 0xf, 0xf, false));
    float s2 = u2f(__builtin_amdgcn_update_dpp(0u, f2u(s1), 0x124, 0xf, 0xf, false));
    float mean = s1 * (1.f / D_);
    OPQ(mean);
    float var = fmaxf(__builtin_fmaf(-mean, mean, s2 * (1.f / D_)), 0.f);
    ms[r] = mean; rsd[r] = __builtin_amdgcn_rsqf(var + 1e-5f);
  }
  if ((lane & 15) < 4) {
    char* d = sm + L_ST + X * 512 + ((lane >> 4) * 16 + (lane & 3) * 4) * 8;
    *(f32x4*)d = f32x4{ms[0], rsd[0], ms[1], rsd[1]};
    *(f32x4*)(d + 16) = f32x4{ms[2], rsd[2], ms[3], rsd[3]};
  }
  asm volatile("" ::: "memory");
  fl[F_SRDY + X] = stage;
}

__device__ __forceinline__ void serve_x(char* ws, char* sm, unsigned smb, lflag fl, int xi, int c, unsigned cnt, unsigned tgt, int lfA, int lfB,
                                        unsigned ltgt, unsigned stage, int lane) {
  u32x4 p[14];
  lwait(fl, lfA, ltgt);
  gpoll(ws, cnt, cnt + 128, tgt);
  dma_x(ws, smb, xi, 8 * c, 0, lane);
  stat_loads(ws, p, xi, 8 * c, lane);
  fl[F_RDY] = stage;
  const bool now = stat_wait_poll(ws, p, cnt + (2 + (lane & 1)) * 128, tgt) && lready(fl, lfB, ltgt);
  if (now) dma_x(ws, smb, xi, 8 * c + 4, 1, lane);
  stat_finish(sm, fl, p, 0, stage, lane);
  if (!now) {
    lwait(fl, lfB, ltgt);
    gpoll(ws, cnt + 256, cnt + 384, tgt);
    dma_x(ws, smb, xi, 8 * c + 4, 1, lane);
  }
  stat_loads(ws, p, xi, 8 * c + 4, lane);
  fl[F_RDY + 1] = stage;
  (void)stat_wait_poll(ws, p, cnt, 0u);
  stat_finish(sm, fl, p, 1, stage, lane);
}

template <class PRE>
__device__ __forceinline__ u32x4 attn_tile(const char* kv, u32x4 q, int qt, int lane, PRE pre) {
  const int i = lane & 15, g = lane >> 4;
  const float NEG = -__builtin_inff();
  f32x4 s[18];
  u32x4 kf[18];
#pragma unroll
  for (int j = 0; j < 6; ++j) kf[j] = lds16(kv + (j >> 1) * 4096 + (j & 1) * 1024);
#pragma unroll
  for (int j = 0; j < 18; ++j) {
    if (j + 6 < 18) kf[j + 6] = lds16(kv + ((j + 6) >> 1) * 4096 + (j & 1) * 1024);
    SCHED();
    s[j] = mfma(kf[j], q, f32x4{0.f, 0.f, 0.f, 0.f});
    if (j % 2 == 1) pre(j / 2);
    SCHED();
  }
  u32x4 vf[18];
#pragma unroll
  for (int j = 0; j < 4; ++j) vf[j] = lds16(kv + (j >> 1) * 4096 + 2048 + (j & 1) * 1024);
  const int dq = 16 * (qt & 1) + i - 4 * g;
#pragma unroll
  for (int u = 0; u < 2; ++u)
#pragma unroll
    for (int r = 0; r < 4; ++r) {
      const bool lo = dq < 16 * u + r;
      s[u][r] = lo ? s[u][r] : NEG;
      s[16 + u][r] = lo ? NEG : s[16 + u][r];
    }
  float mx[4];
#pragma unroll
  for (int r = 0; r < 4; ++r) mx[r] = fmaxf(s[2][r], s[3][r]);
#pragma unroll
  for (int j = 4; j < 20; j += 2)
#pragma unroll
    for (int r = 0; r < 4; ++r) mx[r] = fmaxf(fmaxf(mx[r], s[j % 18][r]), s[(j + 1) % 18][r]);
  const float m = allg_max(fmaxf(fmaxf(mx[0], mx[1]), fmaxf(mx[2], mx[3])));
  constexpr float SC = 0.18898223650461361f * 1.4426950408889634f;
  const float mc = -m * SC;
  const f32x4 m4 = {mc, mc, mc, mc}, c4 = {SC, SC, SC, SC};
  SCHED();
#pragma unroll
  for (int j = 0; j < 18; ++j) s[j] = __builtin_elementwise_fma(s[j], c4, m4);
  pre(9);
  SCHED();
  f32x4 o0 = {0.f, 0.f, 0.f, 0.f}, o1 = {0.f, 0.f, 0.f, 0.f};
#pragma unroll
  for (int j = 0; j < 9; ++j) {
    if (j + 2 < 9) { vf[2 * j + 4] = lds16(kv + (j + 2) * 4096 + 2048); vf[2 * j + 5] = lds16(kv + (j + 2) * 4096 + 3072); }
    float p[8];
#pragma unroll
    for (int k = 0; k < 8; ++k) p[k] = __builtin_amdgcn_exp2f(s[2 * j + (k >> 2)][k & 3]);
    const u32x4 pf = {pk(p[0], p[1]), pk(p[2], p[3]), pk(p[4], p[5]), pk(p[6], p[7])};
    o0 = mfma(vf[2 * j], pf, o0);
    o1 = mfma(vf[2 * j + 1], pf, o1);
    if (j < 6) { pre(10 + 2 * j); pre(11 + 2 * j); }
    SCHED();
  }
  auto r = __builtin_amdgcn_permlane32_swap(f2u(o1[0]), f2u(o1[0]), false, false);
  auto t = __builtin_amdgcn_permlane16_swap(r[1], r[1], false, false);
  const float inv = __builtin_amdgcn_rcpf(u2f(t[1]));
  return u32x4{pk(o0[0] * inv, o0[1] * inv), pk(o0[2] * inv, o0[3] * inv), pk(o1[0] * inv, o1[1] * inv), pk(o1[2] * inv, o1[3] * inv)};
}

__device__ __forceinline__ u32x4 geglu8(const f32x16& acc, f32x2 ms, const f32x4& cg0, const f32x4& cg1, const f32x4& cu0, const f32x4& cu1) {
  const float q = ms.y * ms.y, k1 = GC1 * ms.y, k2 = GC2 * ms.y * q, ir = __builtin_amdgcn_rcpf(q), nm = -ms.x;
  float a[8], b[8], t[8];
#pragma unroll
  for (int r = 0; r < 8; ++r) { a[r] = __builtin_fmaf(nm, r < 4 ? cg0[r & 3] : cg1[r & 3], acc[r]); OPQ(a[r]); }
  SCHED();
#pragma unroll
  for (int r = 0; r < 8; ++r) { t[r] = a[r] * a[r]; OPQ(t[r]); }
  SCHED();
#pragma unroll
  for (int r = 0; r < 8; ++r) { t[r] = __builtin_fmaf(t[r], k2, k1); OPQ(t[r]); }
  SCHED();
#pragma unroll
  for (int r = 0; r < 8; ++r) { t[r] = t[r] * a[r]; OPQ(t[r]); }
  SCHED();
#pragma unroll
  for (int r = 0; r < 8; ++r) {
    t[r] = __builtin_amdgcn_exp2f(t[r]);
    b[r] = __builtin_fmaf(nm, r < 4 ? cu0[r & 3] : cu1[r & 3], acc[r + 8]); OPQ(b[r]);
  }
  SCHED();
#pragma unroll
  for (int r = 0; r < 8; ++r) { t[r] = __builtin_fmaf(t[r], ir, ir); OPQ(t[r]); }
  SCHED();
#pragma unroll
  for (int r = 0; r < 8; ++r) { t[r] = __builtin_amdgcn_rcpf(t[r]); b[r] = a[r] * b[r]; OPQ(b[r]); }
  SCHED();
#pragma unroll
  for (int r = 0; r < 8; ++r) { t[r] = t[r] * b[r]; OPQ(t[r]); }
  SCHED();
  return u32x4{pks(t[0], t[1]), pks(t[2], t[3]), pks(t[4], t[5]), pks(t[6], t[7])};
}

__device__ __forceinline__ void fin5(char* ws, uint16_t* out, char* sm, int X, int nt, int P, int J, int l, int lane) {
  const int i = lane & 15, g = lane >> 4;
  const u32x4 xr = lds16(sm + L_RES + X * 4096 + nt * 1024 + lane * 16);
  const char* red = sm + X * L_REG + nt * 2048 + lane * 16;
  f32x4 pa[7], pb[7];
#pragma unroll
  for (int k = 0; k < 7; ++k) { pa[k] = *(const f32x4*)(red + k * 8192); pb[k] = *(const f32x4*)(red + k * 8192 + 1024); }
  float v[8] = {bflo(xr.x), bfhi(xr.x), bflo(xr.y), bfhi(xr.y), bflo(xr.z), bfhi(xr.z), bflo(xr.w), bfhi(xr.w)};
  SCHED();
#pragma unroll
  for (int k = 0; k < 7; ++k) {
#pragma unroll
    for (int r = 0; r < 8; ++r) { v[r] += r < 4 ? pa[k][r] : pb[k][r - 4]; OPQ(v[r]); }
    SCHED();
  }
  const u32x4 o = {pks(v[0], v[1]), pks(v[2], v[3]), pks(v[4], v[5]), pks(v[6], v[7])};
  if (l == 0) {
    sth(o, ws, (g & 1) * 512 + (i + 16 * (g >> 1)) * 16, OFF_X + 2 * X_BYTES + (P * 28 + J) * KB);
    float s1 = v[0] + v[1], s2 = v[0] * v[0], s3 = v[2] + v[3], s4 = v[1] * v[1];
    OPQ(s1); OPQ(s2); OPQ(s3); OPQ(s4);
    s1 += v[4]; s3 += v[5]; s2 = __builtin_fmaf(v[2], v[2], s2); s4 = __builtin_fmaf(v[3], v[3], s4);
    OPQ(s1); OPQ(s2); OPQ(s3); OPQ(s4);
    s1 += v[6]; s3 += v[7]; s2 = __builtin_fmaf(v[4], v[4], s2); s4 = __builtin_fmaf(v[5], v[5], s4);
    OPQ(s1); OPQ(s2); OPQ(s3); OPQ(s4);
    s1 += s3; s2 = __builtin_fmaf(v[6], v[6], s2); s4 = __builtin_fmaf(v[7], v[7], s4);
    OPQ(s1); OPQ(s2); OPQ(s4);
    s2 += s4;
    auto r = __builtin_amdgcn_permlane32_swap(f2u(s1), f2u(s2), false, false);
    const float sv = u2f(r[0]) + u2f(r[1]);
    sth32(f2u(sv), ws, (g & 1) * 3584 + (lane >> 5) * 64 + i * 4, OFF_ST + 2 * ST_BYTES + (2 * P * 28 + J) * 128);
  } else {
    uint16_t* op = out + (size_t)(P * 32 + (lane & 31)) * D_ + 16 * J + 4 * (lane >> 5);
    *(u32x2*)op = u32x2{o.x, o.y};
    *(u32x2*)(op + 8) = u32x2{o.z, o.w};
  }
}

#define IDS()                                                                                                   \
  const int lane = opaque_v(lane0), wave = opaque(wave0), b = opaque(b0), xcd = b & 7, rk = b >> 3;             \
  const int c1 = MAP1 == 8 ? 2 * xcd + (rk & 1) : MAP1 == 4 ? 4 * (xcd >> 1) + (rk & 3) : 8 * (xcd >> 2) + (rk & 7); \
  const int h1 = MAP1 == 8 ? rk >> 1 : MAP1 == 4 ? 8 * (xcd & 1) + (rk >> 2) : 4 * (xcd & 3) + (rk >> 3);       \
  const int c3 = MAP3 == 8 ? 2 * xcd + (rk & 1) : 4 * (xcd >> 1) + (rk & 3);                                    \
  const int u5 = MAP3 == 8 ? rk >> 1 : 8 * (xcd & 1) + (rk >> 2);                                               \
  const int m3 = (u5 & 3) * 4 + (u5 >> 2);                                                                      \
  const bool act5 = u5 < 14;                                                                                    \
  const int th = u5 & 1, ng = u5 >> 1, i = lane & 15, g = lane >> 4;                                            \
  const int part = wave < 6 ? 2 - (wave >> 1) : 0, nt1 = wave & 1, nt6 = 2 * part + nt1, w7 = min(wave, 6), J4 = 7 * h1 + w7, J3 = 7 * (m3 & 3) + w7; \
  const unsigned l16 = lane * 16, half_off = (lane & 31) * 16, res_off = (i + 16 * (g & 1)) * 16 + 8 * (g >> 1), f32_off = (g & 1) * 512 + (i + 16 * (g >> 1)) * 16; \
  (void)c1; (void)h1; (void)c3; (void)act5; (void)ng; (void)th; (void)part; (void)nt1; (void)J4; (void)J3; (void)l16; (void)half_off; (void)res_off; (void)f32_off; (void)nt6
__global__ __launch_bounds__(NT, 2) void k_main(char* ws, uint16_t* out) {
  extern __shared__ __attribute__((aligned(16))) char sm[];
  const int tid = threadIdx.x, lane0 = tid & 63, wave0 = __builtin_amdgcn_readfirstlane(tid >> 6), b0 = blockIdx.x;
  IDS();
  lflag fl = (lflag)(LAS char*)(sm + L_FL);
  const rsrc_t rs = __builtin_amdgcn_make_buffer_rsrc(ws, 0, WS_USED, 0x00020000);
  int stn = 0; (void)stn;
  const unsigned smb = (unsigned)(uintptr_t)(LAS char*)sm;
  u32x4 w1[14];
  if (wave0 == 7) {
    PRIO(3);
    const unsigned src = OFF_X + 4 * X_BYTES + c1 * 112 * KB + l16;
#pragma unroll
    for (int k = 0; k < 7; ++k) dma8k(ws, src + k * 8192, smb + k * 8192);
    dma1k(ws, OFF_CS + h1 * 2048 + l16, smb + L_CS);
    dma1k(ws, OFF_CS + h1 * 2048 + 1024 + l16, smb + L_CS + 1024);
    fl[lane] = 0u;
    *(f32x2*)(sm + L_ST + lane * 8) = f32x2{0.f, 1.f};
    *(f32x2*)(sm + L_ST + 512 + lane * 8) = f32x2{0.f, 1.f};
    undef<14>(w1);
  } else if (wave0 < 6) {
#pragma unroll
    for (int s = 0; s < 14; ++s) w1[s] = ldwf(rs, l16, OFF_WQKV + (h1 * 6 + nt6) * 14 * KB, s);
  } else {
    undef<14>(w1);
  }
  BAR();

#pragma unroll 1
  for (int l = 0; l < 2; ++l) {
    const unsigned st = 5 * l;
    u32x4 w3[16], w4[28], w5[32];
#define PRE1(k)                                                                                         \
  do {                                                                                                  \
    if ((k) < 16) w3[(k)] = ldwf(rs, l16, OFF_WO + J3 * 16 * KB, (k));                                  \
    else if ((k) < 44) w4[(k) - 16] = ldwf(rs, l16, OFF_WGU + J4 * 28 * KB, (k) - 16);                  \
    else w5[(k) - 44] = ldwf(rs, l16, OFF_WD + ((act5 ? ng : 0) * 112 + 16 * w7) * 2 * KB, (k) - 44);   \
  } while (0)
#define PRE34(k0, k1)                                                                                   \
  SCHED();                                                                                              \
  _Pragma("unroll") for (int k = k0; k < k1; ++k) PRE1(k);                                              \
  SCHED();
    if (wave0 == 7) {
      IDS();
      if (l == 0) {
        const unsigned src = OFF_X + 4 * X_BYTES + (c1 * 112 + 56) * KB + l16;
#pragma unroll
        for (int k = 0; k < 7; ++k) {
          dma8k(ws, src + k * 8192, smb + L_REG + k * 8192);
          if (k == 3) { WAIT_VM(32); fl[F_RDY] = 1u; fl[F_SRDY] = 1u; }
        }
        WAIT_VM(0);
        fl[F_RDY + 1] = 1u; fl[F_SRDY + 1] = 1u;
      } else {
        serve_x(ws, sm, smb, fl, 2, c1, CNT_X2 + 4 * c1 * 128, 7, F_ARR4, (act5 ? F_ARR5 : F_ARR4) + 1, act5 ? 4 : 7, st + 1, lane);
        STAMP();
      }
      lwait(fl, F_ARR1, 6 * (l + 1));
#pragma unroll 1
      for (int k = 0; k < 8 - 4 * c1; ++k) {
        const u32x4 kz = {0u, 0u, g == 3 ? 0xff80u : 0u, 0u}, vz = {0u, 0u, 0u, 0u};
        char* d = sm + L_HALO + k * 4096 + l16;
        *(u32x4*)d = kz; *(u32x4*)(d + 1024) = kz; *(u32x4*)(d + 2048) = vz; *(u32x4*)(d + 3072) = vz;
      }
      if (c1 > 0) gpoll(ws, CNT_KV + ((l * 16 + h1) * 16 + max(c1 - 2, 0)) * 128, CNT_KV + ((l * 16 + h1) * 16 + c1 - 1) * 128, 4);
      STAMP();
      const unsigned src = OFF_KV + l * KV_BYTES + (h1 * 64 + 4 * c1 - 8) * 4096 + l16;
#pragma unroll
      for (int k = 0; k < 4; ++k) if (2 * c1 + k >= 4) dma8k(ws, src + k * 8192, smb + L_HALO + k * 8192);
      WAIT_VM(0);
      PRIO(0);
      undef<6>(w3);
    } else {
      if (wave0 < 6) touch<14>(w1);
      auto half1 = [&](auto x_t) {
        constexpr int X = decltype(x_t)::value;
        IDS();
        if (wave < 6) {
          lwait(fl, F_RDY + X, st + 1);
          STAMP();
          const char* xs = sm + X * L_REG + (g >> 1) * 1024 + half_off;
          const char* stp = sm + L_ST + X * 512;
          const unsigned so_kv = OFF_KV + l * KV_BYTES + (h1 * 64 + 4 * c1 + 2 * X) * 4096;
          f32x4 acc[4];
          u32x4 xf[14][4];
          unsigned sfl;
#pragma unroll
          for (int t = 0; t < 4; ++t) acc[t] = f32x4{0.f, 0.f, 0.f, 0.f};
#define P1_X(s, t) lds16(xs + ((t) >> 1) * 28 * 1024 + (s) * 2048 + ((t) & 1) * 512)
#define P1_LOOP(MM)                                                                                     \
  _Pragma("unroll") for (int s = 0; s < 2; ++s) {                                                       \
    _Pragma("unroll") for (int t = 0; t < 4; ++t) xf[s][t] = P1_X(s, t);                                \
  }                                                                                                     \
  _Pragma("unroll") for (int s = 0; s < 14; ++s) {                                                      \
    if (s + 2 < 14) { _Pragma("unroll") for (int t = 0; t < 4; ++t) xf[s + 2][t] = P1_X(s + 2, t); }    \
    if (s == 12) { sfl = fl[F_SRDY + X]; asm volatile("" ::: "memory"); P1_EPI_LOADS }                  \
    SCHED();                                                                                            \
    _Pragma("unroll") for (int t = 0; t < 4; ++t) acc[t] = MM;                                          \
    if (X == 1 && s >= 2 && s < 8) PRE1(s - 2);                                                         \
    SCHED();                                                                                            \
  }                                                                                                     \
  if ((unsigned)__builtin_amdgcn_readfirstlane(sfl) < st + 1) { lwait(fl, F_SRDY + X, st + 1); P1_EPI_LOADS }
          if (part < 2) {
            f32x4 cs1; f32x2 m[4];
#define P1_EPI_LOADS cs1 = *(const f32x4*)(sm + L_CS + (nt6 * 16 + 4 * g) * 4); _Pragma("unroll") for (int t = 0; t < 4; ++t) m[t] = *(const f32x2*)(stp + t * 128 + i * 8);
            P1_LOOP(mfma(w1[s], xf[s][t], acc[t]))
#undef P1_EPI_LOADS
            STAMP();
            PRIO(2);
            float v[16];
#pragma unroll
            for (int k = 0; k < 16; ++k) { v[k] = __builtin_fmaf(-m[k >> 2].x, cs1[k & 3], acc[k >> 2][k & 3]); OPQ(v[k]); }
            SCHED();
#pragma unroll
            for (int k = 0; k < 16; ++k) { v[k] *= m[k >> 2].y; OPQ(v[k]); }
            SCHED();
            if (nt6 == 1 && g == 3) { v[0] = 1.f; v[4] = 1.f; v[8] = 1.f; v[12] = 1.f; }
            const u32x4 o0 = pair16(u32x2{pk(v[0], v[1]), pk(v[2], v[3])}, u32x2{pk(v[4], v[5]), pk(v[6], v[7])});
            const u32x4 o1 = pair16(u32x2{pk(v[8], v[9]), pk(v[10], v[11])}, u32x2{pk(v[12], v[13]), pk(v[14], v[15])});
            const unsigned off = (g >> 1) * 1024 + nt1 * 512 + half_off;
            if (part == 0) {
              *(u32x4*)(sm + L_Q + X * 4096 + off) = o0;
              *(u32x4*)(sm + L_Q + X * 4096 + 2048 + off) = o1;
            } else {
              sth(o0, ws, off, so_kv); sth(o1, ws, off, so_kv + 4096);
              *(u32x4*)(sm + L_OWN + X * 8192 + off) = o0;
              *(u32x4*)(sm + L_OWN + X * 8192 + 4096 + off) = o1;
            }
          } else {
            float c; f32x4 ma[4], mb[4];
#define P1_EPI_LOADS c = *(const float*)(sm + L_CS + (nt6 * 16 + i) * 4); _Pragma("unroll") for (int t = 0; t < 4; ++t) { ma[t] = *(const f32x4*)(stp + t * 128 + g * 32); mb[t] = *(const f32x4*)(stp + t * 128 + g * 32 + 16); }
            P1_LOOP(mfma(xf[s][t], w1[s], acc[t]))
#undef P1_EPI_LOADS
            STAMP();
            PRIO(2);
            float v[16];
#pragma unroll
            for (int k = 0; k < 16; ++k) { const f32x4 mm = (k & 2) ? mb[k >> 2] : ma[k >> 2]; v[k] = __builtin_fmaf(-mm[2 * (k & 1)], c, acc[k >> 2][k & 3]); OPQ(v[k]); }
            SCHED();
#pragma unroll
            for (int k = 0; k < 16; ++k) { const f32x4 mm = (k & 2) ? mb[k >> 2] : ma[k >> 2]; v[k] *= mm[2 * (k & 1) + 1]; OPQ(v[k]); }
            SCHED();
            u32x4 o0 = {pk(v[0], v[1]), pk(v[2], v[3]), pk(v[4], v[5]), pk(v[6], v[7])};
            u32x4 o1 = {pk(v[8], v[9]), pk(v[10], v[11]), pk(v[12], v[13]), pk(v[14], v[15])};
            if (nt1 == 1 && i == 12) { o0 = u32x4{0x3f803f80u, 0x3f803f80u, 0x3f803f80u, 0x3f803f80u}; o1 = o0; }
            const unsigned off = 2048 + nt1 * 1024 + l16;
            sth(o0, ws, off, so_kv); sth(o1, ws, off, so_kv + 4096);
            *(u32x4*)(sm + L_OWN + X * 8192 + off) = o0;
            *(u32x4*)(sm + L_OWN + X * 8192 + 4096 + off) = o1;
          }
          if (X == 0) arrive_nr(fl, F_ARR1);
          STAMP();
          if (X == 1 && wave < 4) {
            WAIT_VM(0);
            gadd(ws, CNT_KV + ((l * 16 + h1) * 16 + c1) * 128);
            STAMP();
          }
        } else if (X == 1) {
          lwait(fl, F_SRDY + X, st + 1);
          PRE34(0, 6)
        }
        PRIO(0);
      };
      half1(std::integral_constant<int, 0>{});
      half1(std::integral_constant<int, 1>{});
    }
    BAR();
    {
      IDS();
      STAMP();
      const int T = 8 * c1 + wave;
      const u32x4 q = lds16(sm + L_Q + wave * 1024 + l16);
      const char* kv = sm + L_HALO + (wave >> 1) * 4096 + l16;
      const u32x4 o = attn_tile(kv, q, wave, lane, [&](int k) { PRE1(6 + k); });
      sth(o, ws, l16, OFF_O + l * O_BYTES + (T * 16 + h1) * KB);
      STAMP();
      WAIT_VM(0);
      gadd(ws, CNT_O + (l * 128 + T) * 128);
      STAMP();
      arrive_nr(fl, F_GO2);
      if (wave < 7) { lwait(fl, F_GO2, 8 * (l + 1)); PRE34(28, 36) } else { undef<8>(&w4[12]); }
      if (wave == 7) {
        PRIO(3);
        const int P = 4 * c3 + (m3 >> 2);
        gpoll(ws, CNT_O + (l * 128 + 2 * P) * 128, CNT_O + (l * 128 + 2 * P + 1) * 128, 16);
        STAMP();
        const unsigned src = OFF_O + l * O_BYTES + P * 32 * KB + l16;
#pragma unroll
        for (int k = 0; k < 4; ++k) dma8k(ws, src + k * 8192, smb + L_REG + k * 8192);
        dma8k(ws, OFF_X + 2 * l * X_BYTES + (P * 28 + 7 * (m3 & 3)) * KB + l16, smb + L_RES);
        WAIT_VM(0);
      }
    }
    BAR();
    if (wave0 == 7) {
      IDS();
      serve_x(ws, sm, smb, fl, 2 * l + 1, c1, CNT_X1 + (l * 64 + 4 * c1) * 128, 4, F_ARR3, F_ARR3, 7 * (l + 1), st + 4, lane);
      STAMP();
      if (act5) {
        const unsigned cH = CNT_H + (l * 16 + c3) * 2 * 128;
        const int P = 4 * c3 + th;
        const unsigned rsrc = OFF_X + (2 * l + 1) * X_BYTES + (P * 28 + 4 * ng) * KB + f32_off;
        lwait(fl, F_ARR4 + 1, 7 * l + 4);
        gpoll(ws, cH, cH, 16);
        fl[F_RDY] = st + 5;
        lwait(fl, F_GO4, l + 1);
        STAMP();
        __builtin_amdgcn_s_sleep(8);
        gpoll(ws, cH + 128, cH + 128, 16);
        fl[F_RDY + 1] = st + 5;
        STAMP();
        dma4k(ws, rsrc, smb + L_RES);
        dma4k(ws, rsrc + 56 * KB, smb + L_RES + 4096);
        WAIT_VM(0);
        arrive_nr(fl, F_RED + 1);
        lwait(fl, F_RED, 7 * (l + 1));
#pragma unroll 1
        for (int nt = 0; nt < 4; ++nt) fin5(ws, out, sm, 0, nt, P, 4 * ng + nt, l, lane);
        if (l == 0) { WAIT_VM(0); gadd(ws, CNT_X2 + P * 128); }
        STAMP();
      }
      undef<14>(w1);
    } else {
      {
        IDS();
        STAMP();
        const char* os = sm + L_REG + l16;
        u32x4 oa[16], ob[16];
#pragma unroll
        for (int s = 0; s < 6; ++s) { oa[s] = lds16(os + s * 1024); ob[s] = lds16(os + (16 + s) * 1024); }
        const u32x2 xa = *(const u32x2*)(sm + L_RES + wave * 1024 + res_off), xb = *(const u32x2*)(sm + L_RES + wave * 1024 + 512 + res_off);
        f32x4 a0 = {0.f, 0.f, 0.f, 0.f}, a1 = {0.f, 0.f, 0.f, 0.f};
#pragma unroll
        for (int s = 0; s < 16; ++s) {
          SCHED();
          a0 = mfma(w3[s], oa[s], a0); a1 = mfma(w3[s], ob[s], a1);
          if (s < 8) PRE1(36 + s);
          SCHED();
          if (s + 6 < 16) { oa[s + 6] = lds16(os + (s + 6) * 1024); ob[s + 6] = lds16(os + (22 + s) * 1024); }
        }
        PRIO(2);
        float v[8] = {a0[0] + bflo(xa.x), a0[1] + bfhi(xa.x), a0[2] + bflo(xa.y), a0[3] + bfhi(xa.y),
                      a1[0] + bflo(xb.x), a1[1] + bfhi(xb.x), a1[2] + bflo(xb.y), a1[3] + bfhi(xb.y)};
#pragma unroll
        for (int k = 0; k < 8; ++k) OPQ(v[k]);
        const int P = 4 * c3 + (m3 >> 2);
        sth(pair16(u32x2{pk(v[0], v[1]), pk(v[2], v[3])}, u32x2{pk(v[4], v[5]), pk(v[6], v[7])}), ws, l16,
            OFF_X + (2 * l + 1) * X_BYTES + (P * 28 + J3) * KB);
        float s1a = v[0] + v[1], s1b = v[4] + v[5], s2a = v[0] * v[0], s2b = v[4] * v[4];
        OPQ(s1a); OPQ(s1b); OPQ(s2a); OPQ(s2b);
        s1a += v[2]; s1b += v[6]; s2a = __builtin_fmaf(v[1], v[1], s2a); s2b = __builtin_fmaf(v[5], v[5], s2b);
        OPQ(s1a); OPQ(s1b); OPQ(s2a); OPQ(s2b);
        s1a += v[3]; s1b += v[7]; s2a = __builtin_fmaf(v[2], v[2], s2a); s2b = __builtin_fmaf(v[6], v[6], s2b);
        OPQ(s2a); OPQ(s2b);
        s2a = __builtin_fmaf(v[3], v[3], s2a); s2b = __builtin_fmaf(v[7], v[7], s2b);
        auto r1 = __builtin_amdgcn_permlane32_swap(f2u(s1a), f2u(s1b), false, false);
        auto r2 = __builtin_amdgcn_permlane32_swap(f2u(s2a), f2u(s2b), false, false);
        float s1 = u2f(r1[0]) + u2f(r1[1]), s2 = u2f(r2[0]) + u2f(r2[1]);
        OPQ(s1); OPQ(s2);
        auto t = __builtin_amdgcn_permlane16_swap(f2u(s1), f2u(s2), false, false);
        sth32(f2u(u2f(t[0]) + u2f(t[1])), ws, (g >> 1) * 3584 + (lane & 31) * 4, OFF_ST + (2 * l + 1) * ST_BYTES + (2 * P * 28 + J3) * 128);
        STAMP();
        WAIT_VM(0);
        if (arrive(fl, F_ARR3, 7 * (l + 1))) { gadd(ws, CNT_X1 + (l * 64 + P) * 128); arrive_nr(fl, F_GO3); }
        lwait(fl, F_GO3, l + 1);
        PRE34(44, 52)

        PRIO(0);
        STAMP();
      }
      u32x4 hf[2][16];
      auto half4 = [&](auto x_t) {
        constexpr int X = decltype(x_t)::value;
        IDS();
        const char* xs = sm + X * L_REG + f32_off;
        const char* stp = sm + L_ST + X * 512 + (lane & 31) * 8;
        const char* csp = sm + L_CS + (96 + wave * 16 + 4 * (lane >> 5)) * 4;
        f32x16 a0, a1;
        u32x4 xa[28], xb[28];
        f32x2 m0, m1; f32x4 cg0, cg1, cu0, cu1;
        unsigned sfl;
        lwait(fl, F_RDY + X, st + 4);
        STAMP();
        PRIO(1 - X);
#define P4_LOOP(STREAM)                                                                                 \
  _Pragma("unroll") for (int r = 0; r < 16; ++r) { a0[r] = 0.f; a1[r] = 0.f; }                          \
  _Pragma("unroll") for (int s = 0; s < 3; ++s) { xa[s] = lds16(xs + s * 1024); xb[s] = lds16(xs + (28 + s) * 1024); } \
  _Pragma("unroll") for (int s = 0; s < 28; ++s) {                                                      \
    if (s + 3 < 28) { xa[s + 3] = lds16(xs + (s + 3) * 1024); xb[s + 3] = lds16(xs + (31 + s) * 1024); } \
    if (s == 25) { sfl = fl[F_SRDY + X]; asm volatile("" ::: "memory"); m0 = *(const f32x2*)stp; m1 = *(const f32x2*)(stp + 256); } \
    if (s == 26) { cg0 = *(const f32x4*)csp; cg1 = *(const f32x4*)(csp + 32); }                         \
    if (s == 27) { cu0 = *(const f32x4*)(csp + 448); cu1 = *(const f32x4*)(csp + 480); }                \
    if (X == 0 && s >= 16 && (s & 1) == 0) PRE1(44 + (s >> 1));                                         \
    SCHED();                                                                                            \
    a0 = mfma32(w4[s], xa[s], a0); a1 = mfma32(w4[s], xb[s], a1);                                       \
    SCHED();                                                                                            \
    if (X == 1 && s == 4 && wave < 4) {                                                                 \
      WAIT_VM(0);                                                                                       \
      if (arrive(fl, F_ARR4, 7 * (l + 1))) gadd(ws, CNT_H + ((l * 16 + c1) * 2) * 128);                 \
    }                                                                                                   \
    if (X == 1 && s >= 5 && s < 23) w5[9 + s] = ldwf(rs, vo5, so5, 9 + s);                              \
    SCHED();                                                                                            \
  }
        const unsigned so5 = OFF_WD + ((act5 ? ng : 0) * 112 + 16 * wave) * 2 * KB, vo5 = opaque_v(l16 + (wave < 4 ? 0u : 0x40000000u));
        P4_LOOP(0)
        if ((unsigned)__builtin_amdgcn_readfirstlane(sfl) < st + 4) {
          lwait(fl, F_SRDY + X, st + 4);
          m0 = *(const f32x2*)stp; m1 = *(const f32x2*)(stp + 256);
        }
        STAMP();
        PRIO(2);
        const unsigned so = OFF_H + l * H_BYTES + ((4 * c1 + 2 * X) * 112 + J4) * KB;
        sth(geglu8(a0, m0, cg0, cg1, cu0, cu1), ws, f32_off, so);
        sth(geglu8(a1, m1, cg0, cg1, cu0, cu1), ws, f32_off, so + 112 * KB);
        STAMP();
        SCHED();
        if (X == 0) {
          if (wave >= 4) {
            WAIT_VM(0);
            if (arrive(fl, F_ARR4, 7 * (l + 1))) gadd(ws, CNT_H + ((l * 16 + c1) * 2) * 128);
          }
        } else {
          WAIT_VM(0);
          if (arrive(fl, F_ARR4 + 1, 7 * (l + 1))) { gadd(ws, CNT_H + ((l * 16 + c1) * 2 + 1) * 128); arrive_nr(fl, F_GO4); }
          lwait(fl, F_GO4, l + 1);
          if (wave >= 4) { PRE34(58, 76) }
          if (act5) lwait(fl, F_RDY + 0, st + 5);
          SCHED();
          const unsigned soh = act5 ? OFF_H + l * H_BYTES + ((4 * c3 + th) * 112 + 16 * wave) * KB : OFF_WD;
#pragma unroll
          for (int s = 0; s < 16; ++s) hf[0][s] = ldhf(rs, f32_off, soh, s);
          SCHED();
          PRIO(0);
          STAMP();
        }
      };
      half4(std::integral_constant<int, 0>{});
      half4(std::integral_constant<int, 1>{});
      SCHED();
      auto mma5 = [&](auto x_t) {
        constexpr int X = decltype(x_t)::value;
        IDS();
        const unsigned soh = OFF_H + l * H_BYTES + ((4 * c3 + 2 + th) * 112 + 16 * wave) * KB;
        f32x16 a0, a1;
#pragma unroll
        for (int r = 0; r < 16; ++r) { a0[r] = 0.f; a1[r] = 0.f; }
#pragma unroll
        for (int s = 0; s < 16; ++s) {
          SCHED();
          a0 = mfma32(w5[2 * s], hf[X][s], a0); a1 = mfma32(w5[2 * s + 1], hf[X][s], a1);
          SCHED();
          if (X == 0) hf[1][s] = ldhf(rs, f32_off, soh, s);
        }
        SCHED();
        char* mine = sm + X * L_REG + wave * 8192 + l16;
#pragma unroll
        for (int k = 0; k < 4; ++k) {
          *(f32x4*)(mine + k * 1024) = f32x4{a0[4 * k], a0[4 * k + 1], a0[4 * k + 2], a0[4 * k + 3]};
          *(f32x4*)(mine + (4 + k) * 1024) = f32x4{a1[4 * k], a1[4 * k + 1], a1[4 * k + 2], a1[4 * k + 3]};
        }
        arrive_nr(fl, F_RED + X);
      };
      if (act5) {
        lwait(fl, F_RDY + 1, st + 5);
        STAMP();
        PRIO(1);
        mma5(std::integral_constant<int, 0>{});
        PRIO(0);
        STAMP();
        mma5(std::integral_constant<int, 1>{});
        STAMP();
      }
      SCHED();
      if (l == 0 && wave0 < 6) {
        IDS();
#pragma unroll
        for (int s = 0; s < 14; ++s) w1[s] = ldwf(rs, l16, OFF_WQKV + (h1 * 6 + nt6) * 14 * KB, s);
      } else {
        undef<14>(w1);
      }
      SCHED();
      if (act5 && wave0 < 4) {
        IDS();
        PRIO(2);
        lwait(fl, F_RED + 1, 8 * (l + 1));
        fin5(ws, out, sm, 1, wave, 4 * c3 + 2 + th, 4 * ng + wave, l, lane);
        STAMP();
        if (l == 0) {
          WAIT_VM(0);
          if (arrive(fl, F_ARR5 + 1, 4)) gadd(ws, CNT_X2 + (4 * c3 + 2 + th) * 128);
        }
        PRIO(0);
        STAMP();
      }
    }
  }
}

extern "C" void kernel_launch(void* const* d_in, const int* in_sizes, int n_in, void* d_out, int out_size, void* d_ws,
                              size_t ws_size, hipStream_t stream) {
  const uint16_t* x = (const uint16_t*)d_in[0];
  const uint16_t* wqkv = (const uint16_t*)d_in[1];
  const uint16_t* wo = (const uint16_t*)d_in[2];
  const uint16_t* wg = (const uint16_t*)d_in[4];
  const uint16_t* wu = (const uint16_t*)d_in[5];
  const uint16_t* wd = (const uint16_t*)d_in[6];
  char* ws = (char*)d_ws;
  (void)hipFuncSetAttribute((const void*)k_main, hipFuncAttributeMaxDynamicSharedMemorySize, L_BYTES);
  k_prep<<<256, NT, 0, stream>>>(x, wqkv, wo, wg, wu, wd, ws);
  k_main<<<256, NT, L_BYTES, stream>>>(ws, (uint16_t*)d_out);
}
```

```cpp
#include <hip/hip_runtime.h>
#include <hip/hip_bf16.h>
#include <stdint.h>
#include <type_traits>

typedef short bf16x8 __attribute__((ext_vector_type(8)));
typedef float f32x4 __attribute__((ext_vector_type(4)));
typedef float f32x2 __attribute__((ext_vector_type(2)));
typedef float f32x16 __attribute__((ext_vector_type(16)));
typedef unsigned u32x4 __attribute__((ext_vector_type(4)));
typedef unsigned u32x2 __attribute__((ext_vector_type(2)));
typedef __bf16 bf16x2_t __attribute__((ext_vector_type(2)));
#define GAS __attribute__((address_space(1)))
#define LAS __attribute__((address_space(3)))
typedef GAS unsigned gu32;
typedef volatile LAS unsigned* lflag;
#define RLX_AGENT __ATOMIC_RELAXED, __HIP_MEMORY_SCOPE_AGENT

constexpr int D_ = 448, DH = 28, F_ = 1792;
constexpr int NT = 512;
constexpr unsigned KB = 1024;

constexpr unsigned CNT_X2 = 0;
constexpr unsigned CNT_O  = CNT_X2 + 64 * 128;
constexpr unsigned CNT_X1 = CNT_O + 256 * 128;
constexpr unsigned CNT_H  = CNT_X1 + 128 * 128;
constexpr unsigned CNT_KV = CNT_H + 64 * 128;
constexpr unsigned NCTRL  = CNT_KV / 128 + 512;
static_assert(NCTRL == 1024, "k_prep clears four lines per workgroup");
constexpr unsigned OFF_WQKV = 128 * KB;
constexpr unsigned OFF_WO   = OFF_WQKV + 16 * 6 * 14 * KB;
constexpr unsigned OFF_WGU  = OFF_WO + 28 * 16 * KB;
constexpr unsigned OFF_WD   = OFF_WGU + 112 * 28 * KB;
constexpr unsigned OFF_CS   = OFF_WD + 7 * 56 * 4 * KB;
constexpr unsigned X_BYTES  = 128 * 28 * 512;
constexpr unsigned OFF_X    = OFF_CS + 32 * KB;
constexpr unsigned ST_BYTES = 128 * 28 * 128;
constexpr unsigned OFF_ST   = OFF_X + 5 * X_BYTES;
constexpr unsigned KV_BYTES = 16 * 64 * 4 * KB;
constexpr unsigned OFF_KV   = OFF_ST + 4 * ST_BYTES;
constexpr unsigned O_BYTES  = 128 * 16 * KB;
constexpr unsigned OFF_O    = OFF_KV + 2 * KV_BYTES;
constexpr unsigned H_BYTES  = 128 * 56 * KB;
constexpr unsigned OFF_H    = OFF_O + 2 * O_BYTES;
constexpr unsigned WS_USED  = OFF_H + 2 * H_BYTES;
constexpr unsigned OFF_STAMP = 64u << 20;

constexpr int L_REG = 80 * 1024;
constexpr int L_HALO = 24 * 1024, L_OWN = 56 * 1024, L_Q = 72 * 1024;
constexpr int L_RES = 136 * 1024;
constexpr int L_ST = L_RES + 8192;
constexpr int L_CS = L_ST + 1024;
constexpr int L_FL = L_CS + 2048;
constexpr int L_STAMP = L_FL + 256;
constexpr int L_BYTES = L_STAMP + 8 * 128 * 4;
enum { F_RDY = 0, F_SRDY = 2, F_ARR1 = 4, F_ARR3 = 5, F_ARR4 = 6, F_ARR5 = 8, F_RED = 10, F_ABORT = 12, F_GO2 = 13, F_GO3 = 14, F_GO4 = 15, F_GO5 = 16 };

__device__ __forceinline__ f32x4 mfma(u32x4 a, u32x4 b, f32x4 c) {
  return __builtin_amdgcn_mfma_f32_16x16x32_bf16(__builtin_bit_cast(bf16x8, a), __builtin_bit_cast(bf16x8, b), c, 0, 0, 0);
}
__device__ __forceinline__ f32x16 mfma32(u32x4 a, u32x4 b, f32x16 c) {
  return __builtin_amdgcn_mfma_f32_32x32x16_bf16(__builtin_bit_cast(bf16x8, a), __builtin_bit_cast(bf16x8, b), c, 0, 0, 0);
}
__device__ __forceinline__ unsigned pk(float a, float b) {
  bf16x2_t v = {(__bf16)a, (__bf16)b};
  return __builtin_bit_cast(unsigned, v);
}
__device__ __forceinline__ unsigned pks(float a, float b) {
  unsigned r;
  asm("v_cvt_pk_bf16_f32 %0, %1, %2" : "=v"(r) : "v"(a), "v"(b));
  return r;
}
__device__ __forceinline__ float bflo(unsigned u) { return __builtin_bit_cast(float, u << 16); }
__device__ __forceinline__ float bfhi(unsigned u) { return __builtin_bit_cast(float, u & 0xffff0000u); }
__device__ __forceinline__ unsigned f2u(float f) { return __builtin_bit_cast(unsigned, f); }
__device__ __forceinline__ float u2f(unsigned u) { return __builtin_bit_cast(float, u); }
__device__ __forceinline__ u32x4 lds16(const char* p) { return *(const u32x4*)p; }
#define FW(a, b) (a)
#define MAP1 4
#define MAP3 8
#define POLL_SLEEP "0"
#define SCHED() __builtin_amdgcn_sched_barrier(0)
#define OPQ(x) asm("" : "+v"(x))
#define PRIO(n) __builtin_amdgcn_s_setprio(n)
#define WAIT_VM(n) asm volatile("s_waitcnt vmcnt(" #n ")" ::: "memory")
#define WAIT_ALL() asm volatile("s_waitcnt vmcnt(0) lgkmcnt(0)" ::: "memory")
#define WAIT_LGKM() asm volatile("s_waitcnt lgkmcnt(0)" ::: "memory")
typedef __amdgpu_buffer_rsrc_t rsrc_t;
__device__ __forceinline__ u32x4 ldw(rsrc_t rs, unsigned voff, unsigned soff) { return __builtin_amdgcn_raw_buffer_load_b128(rs, voff, soff, 0); }
__device__ __forceinline__ u32x4 ldh(rsrc_t rs, unsigned voff, unsigned soff) { return __builtin_amdgcn_raw_buffer_load_b128(rs, voff, soff, 16); }
__device__ __forceinline__ u32x4 ldwf(rsrc_t rs, unsigned l16, unsigned base, int k) { return ldw(rs, l16 + (k & 3) * 1024, base + (k >> 2) * 4096); }
#define WCNT(n) #n
#define WAIT_W(n) asm volatile("s_waitcnt vmcnt(" WCNT(n) ") lgkmcnt(0)" ::: "memory")
__device__ __forceinline__ u32x4 ldhf(rsrc_t rs, unsigned l16, unsigned base, int k) { return ldh(rs, l16 + (k & 3) * 1024, base + (k >> 2) * 4096); }
__device__ __forceinline__ unsigned opaque(unsigned v) { asm volatile("" : "+s"(v)); return v; }
template <int N> __device__ __forceinline__ void touch(const u32x4* w) {
#pragma unroll
  for (int k = 0; k < N; ++k) asm volatile("" :: "v"(w[k]));
}
template <int N> __device__ __forceinline__ void undef(u32x4* w) {
#pragma unroll
  for (int k = 0; k < N; ++k) asm volatile("" : "=v"(w[k]));
}
__device__ __forceinline__ int opaque_v(int v) { asm volatile("" : "+v"(v)); return v; }
__device__ __forceinline__ void sth(u32x4 v, char* ws, unsigned voff, unsigned soff) {
  asm volatile("global_store_dwordx4 %0, %1, %2 sc1\n\ts_nop 1" :: "v"(voff + soff), "v"(v), "s"(ws) : "memory");
}
__device__ __forceinline__ void sth32(unsigned v, char* ws, unsigned voff, unsigned soff) {
  asm volatile("global_store_dword %0, %1, %2 sc1" :: "v"(voff + soff), "v"(v), "s"(ws) : "memory");
}

#define STAMP() do {} while (0)

typedef short s16x4 __attribute__((ext_vector_type(4)));
__device__ __forceinline__ u32x2 ldtr(const char* p) {
  return __builtin_bit_cast(u32x2, __builtin_amdgcn_ds_read_tr16_b64_v4i16((LAS s16x4*)(LAS char*)p));
}
__device__ __forceinline__ float rbf(float v) { return bflo(pk(v, v) & 0xffffu); }
__device__ __forceinline__ float sum8(u32x4 v) {
  return ((bflo(v.x) + bfhi(v.x)) + (bflo(v.y) + bfhi(v.y))) + ((bflo(v.z) + bfhi(v.z)) + (bflo(v.w) + bfhi(v.w)));
}
__device__ __forceinline__ float dpp_add(float v, const int ctrl) {
  switch (ctrl) {
    case 0: return v + u2f(__builtin_amdgcn_update_dpp(0u, f2u(v), 0xB1, 0xf, 0xf, false));
    case 1: return v + u2f(__builtin_amdgcn_update_dpp(0u, f2u(v), 0x4E, 0xf, 0xf, false));
    case 2: return v + u2f(__builtin_amdgcn_update_dpp(0u, f2u(v), 0x124, 0xf, 0xf, false));
    default: return v + u2f(__builtin_amdgcn_update_dpp(0u, f2u(v), 0x128, 0xf, 0xf, false));
  }
}
__device__ __forceinline__ float sum32(float v) { auto r = __builtin_amdgcn_permlane32_swap(f2u(v), f2u(v), false, false); return u2f(r[0]) + u2f(r[1]); }
__device__ __forceinline__ float sum16(float v) { auto r = __builtin_amdgcn_permlane16_swap(f2u(v), f2u(v), false, false); return u2f(r[0]) + u2f(r[1]); }
__device__ __forceinline__ float wave_sum(float v) { return sum32(sum16(dpp_add(dpp_add(dpp_add(dpp_add(v, 0), 1), 2), 3))); }

#define PST16(v, off) sth((v), ws, (off), 0u)
constexpr int MAIN_LINES = 267;
constexpr int PW_STRIDE = 80, PW_BYTES = 452 * PW_STRIDE, PX_OFF = PW_BYTES, PR_OFF = PX_OFF + 2 * 7168;
__global__ __launch_bounds__(NT) void k_prep(const uint16_t* __restrict__ x, const uint16_t* __restrict__ wqkv,
                                             const uint16_t* __restrict__ wo, const uint16_t* __restrict__ wg,
                                             const uint16_t* __restrict__ wu, const uint16_t* __restrict__ wd, char* ws) {
  __shared__ __attribute__((aligned(16))) char sm[PR_OFF + 8 * 32 * 4];
  const int b = blockIdx.x, tid = threadIdx.x, lane = tid & 63, wave = __builtin_amdgcn_readfirstlane(tid >> 6);
  if (tid < 4) *(unsigned*)(ws + (b * 4 + tid) * 128) = 0u;
  unsigned code_word;
  {
    const char* pc; int delta;
    asm volatile("s_getpc_b64 %0\n1:\n\ts_mov_b32 %1, _Z6k_mainPcPt-1b" : "=s"(pc), "=s"(delta));
    const char* code = pc + delta;
    code_word = *(const GAS unsigned*)(code + min(tid, MAIN_LINES - 1) * 128);
  }
  const u32x4 xv = *(const u32x4*)(x + (size_t)(b * 8 + wave) * D_ + min(lane, 55) * 8);
  const uint16_t* src = wo; int ld = D_, ncols = 32, type = 3; char* dst = ws; int csf = -1, csb = 0, s_stride = 0, nt_stride = 0;
  if (b < 48) {
    int h = b / 3, p = b % 3; type = 0;
    src = wqkv + p * D_ + DH * h; ld = 3 * D_; ncols = DH;
    dst = ws + OFF_WQKV + (size_t)(h * 3 + p) * 28 * KB; s_stride = 1024; nt_stride = 14 * 1024;
    csf = h * 112 + p * 32;
  } else if (b < 62) {
    int j = b - 48; type = 1;
    src = wo + 32 * j;
    dst = ws + OFF_WO + (size_t)(2 * j) * 16 * KB; s_stride = 1024; nt_stride = 16 * 1024;
  } else if (b < 174) {
    int j = (b - 62) >> 1, u = (b - 62) & 1; type = 2;
    src = (u ? wu : wg) + 32 * j; ld = F_;
    dst = ws + OFF_WGU + (size_t)(2 * j) * 28 * KB + u * 256; s_stride = 1024; nt_stride = 28 * 1024;
    csf = 32 * j; csb = 96 + 112 * u;
  } else if (b < 230) {
    int j = (b - 174) >> 2, q = (b - 174) & 3; type = 2;
    src = wd + (size_t)q * 448 * D_ + 32 * j;
    dst = ws + OFF_WD + ((size_t)(j >> 1) * 112 + q * 28) * 2 * KB + (j & 1) * KB; s_stride = 2048; nt_stride = 256;
  }
  u32x2 wv[7];
  const int c4 = tid & 7;
  const bool cok = c4 * 4 < ncols;
#pragma unroll
  for (int u = 0; u < 7; ++u) wv[u] = *(const u32x2*)(src + (size_t)(u * 64 + (tid >> 3)) * ld + (cok ? c4 * 4 : 0));
  {
    const float e[8] = {bflo(xv.x), bfhi(xv.x), bflo(xv.y), bfhi(xv.y), bflo(xv.z), bfhi(xv.z), bflo(xv.w), bfhi(xv.w)};
    const float mean = wave_sum(lane < 56 ? sum8(xv) : 0.f) * (1.f / D_);
    float q = 0.f;
#pragma unroll
    for (int k = 0; k < 8; ++k) q += (e[k] - mean) * (e[k] - mean);
    const float var = wave_sum(lane < 56 ? q : 0.f) * (1.f / D_);
    const float mb = rbf(mean), sd = rbf(__builtin_sqrtf(rbf(rbf(var) + 1e-5f)));
    const float isd = 1.f / sd;
    float y[8];
#pragma unroll
    for (int k = 0; k < 8; ++k) y[k] = rbf(e[k] - mb) * isd;
    if (lane < 56) {
      char* d = sm + PX_OFF + (lane >> 1) * 256 + wave * 16 + (lane & 1) * 8;
      *(u32x2*)d = u32x2{xv.x, xv.y}; *(u32x2*)(d + 128) = u32x2{xv.z, xv.w};
      *(u32x2*)(d + 7168) = u32x2{pk(y[0], y[1]), pk(y[2], y[3])}; *(u32x2*)(d + 7168 + 128) = u32x2{pk(y[4], y[5]), pk(y[6], y[7])};
    }
  }
#pragma unroll
  for (int u = 0; u < 7; ++u) *(u32x2*)(sm + (u * 64 + (tid >> 3)) * PW_STRIDE + c4 * 8) = cok ? wv[u] : u32x2{0u, 0u};
  if (tid < 40) *(u32x2*)(sm + 448 * PW_STRIDE + tid * 8) = u32x2{0u, 0u};
  asm volatile("" :: "v"(code_word));
  __syncthreads();
  if (tid < 448) {
    const unsigned o = ((b >> 2) * 28 + (tid >> 4)) * KB + ((b >> 1) & 1) * 512 + ((tid >> 3) & 1) * 256 + (b & 1) * 128 + (tid & 7) * 16;
    PST16(lds16(sm + PX_OFF + tid * 16), OFF_X + o);
    PST16(lds16(sm + PX_OFF + 7168 + tid * 16), OFF_X + 4 * X_BYTES + o);
  }
  if (type == 3) return;
  const int i = lane & 15, g = lane >> 4, q4 = i >> 2, p4 = i & 3;
  float csum = 0.f;
#pragma unroll
  for (int k = 0; k < 4; ++k) {
    const int f = wave + 8 * k;
    if (f < (type == 1 ? 32 : 28)) {
      int s, nt, r0, r1;
      if (type == 2) { s = f; nt = g & 1; r0 = 16 * s + 4 * (g >> 1); r1 = r0 + 8; }
      else if (type == 0) { s = f >> 1; nt = f & 1; r0 = 32 * s + 16 * (g >> 1) + 4 * (g & 1); r1 = r0 + 8; }
      else { s = f >> 1; nt = f & 1; r0 = DH * s + 4 * g; r1 = g < 3 ? r0 + 16 : 448; }
      const u32x2 lo = ldtr(sm + (r0 + q4) * PW_STRIDE + nt * 32 + p4 * 8), hi = ldtr(sm + (r1 + q4) * PW_STRIDE + nt * 32 + p4 * 8);
      const u32x4 o = {lo.x, lo.y, hi.x, hi.y};
      PST16(o, (unsigned)(dst - ws) + s * s_stride + nt * nt_stride + (type == 2 ? i * 16 + (g >> 1) * 512 : lane * 16));
      csum += sum8(o);
    }
  }
  if (csf >= 0) {
    csum = sum32(csum);
    if (type == 0) csum = sum16(csum);
    float* red = (float*)(sm + PR_OFF);
    if (lane < 32) red[wave * 32 + lane] = (type == 0 && (lane >> 4) != (wave & 1)) ? 0.f : csum;
    __syncthreads();
    if (tid < 32) {
      float t = 0.f;
#pragma unroll
      for (int w = 0; w < 8; ++w) t += red[w * 32 + tid];
      const int f = csf + tid;
      ((float*)(ws + OFF_CS))[(f / 112) * 512 + csb + f % 112] = t;
    }
  }
}

__device__ __forceinline__ void lwait(lflag fl, int idx, unsigned tgt) {
  unsigned v, sv, n = 1u << 16;
  const unsigned addr = (unsigned)(uintptr_t)(fl + idx);
  asm volatile("1:\n\tds_read_b32 %0, %3\n\ts_waitcnt lgkmcnt(0)\n\tv_readfirstlane_b32 %1, %0\n\ts_cmp_ge_u32 %1, %4\n\t"
               "s_cbranch_scc1 2f\n\ts_sleep 1\n\ts_sub_u32 %2, %2, 1\n\ts_cbranch_scc0 1b\n2:"
               : "=&v"(v), "=&s"(sv), "+s"(n) : "v"(addr), "s"(tgt) : "memory", "scc");
}
__device__ __forceinline__ bool lready(lflag fl, int idx, unsigned tgt) {
  bool r = (unsigned)__builtin_amdgcn_readfirstlane(fl[idx]) >= tgt;
  asm volatile("" ::: "memory");
  return r;
}
__device__ __forceinline__ bool arrive(lflag fl, int idx, unsigned tgt) {
  unsigned old, so;
  const unsigned addr = (unsigned)(uintptr_t)(fl + idx), one = 1u;
  asm volatile("s_mov_b64 exec, 1\n\tds_add_rtn_u32 %0, %2, %3\n\ts_mov_b64 exec, -1\n\ts_waitcnt lgkmcnt(0)\n\tv_readfirstlane_b32 %1, %0"
               : "=&v"(old), "=s"(so) : "v"(addr), "v"(one) : "memory");
  return so + 1u == tgt;
}
__device__ __forceinline__ void arrive_nr(lflag fl, int idx) {
  const unsigned addr = (unsigned)(uintptr_t)(fl + idx), one = 1u;
  asm volatile("s_mov_b64 exec, 1\n\tds_add_u32 %0, %1\n\ts_mov_b64 exec, -1" :: "v"(addr), "v"(one) : "memory");
}
__device__ __forceinline__ void gadd(char* ws, unsigned off) {
  const unsigned one = 1u;
  asm volatile("s_mov_b64 exec, 1\n\tglobal_atomic_add %0, %1, %2\n\ts_mov_b64 exec, -1" :: "v"(off), "v"(one), "s"(ws) : "memory");
}
__device__ __forceinline__ void gpoll(char* ws, unsigned off0, unsigned off1, unsigned tgt) {
  unsigned a, c, n = 1u << 15;
  off0 = __builtin_amdgcn_readfirstlane(off0); off1 = __builtin_amdgcn_readfirstlane(off1);
  asm volatile("s_nop 4\n1:\n\ts_load_dword %0, %3, %4 glc\n\ts_load_dword %1, %3, %5 glc\n\ts_waitcnt lgkmcnt(0)\n\ts_min_u32 %0, %0, %1\n\t"
               "s_cmp_ge_u32 %0, %6\n\ts_cbranch_scc1 2f\n\ts_sleep " POLL_SLEEP "\n\ts_sub_u32 %2, %2, 1\n\ts_cbranch_scc0 1b\n2:"
               : "=&s"(a), "=&s"(c), "+s"(n) : "s"(ws), "s"(off0), "s"(off1), "s"(tgt) : "memory", "scc");
}
__device__ __forceinline__ bool gready(char* ws, unsigned off, unsigned tgt) {
  unsigned a;
  off = __builtin_amdgcn_readfirstlane(off);
  asm volatile("s_nop 4\n\ts_load_dword %0, %1, %2 glc\n\ts_waitcnt lgkmcnt(0)" : "=&s"(a) : "s"(ws), "s"(off) : "memory");
  return a >= tgt;
}
__device__ __forceinline__ void warm(const char* ws, unsigned off, int nlines, int lane) {
  unsigned v, a[7];
#pragma unroll
  for (int k = 0; k < 7; ++k) a[k] = off + min(k * 64 + lane, nlines - 1) * 128;
  asm volatile("global_load_dword %0, %1, %8 sc1\n\tglobal_load_dword %0, %2, %8 sc1\n\tglobal_load_dword %0, %3, %8 sc1\n\t"
               "global_load_dword %0, %4, %8 sc1\n\tglobal_load_dword %0, %5, %8 sc1\n\tglobal_load_dword %0, %6, %8 sc1\n\t"
               "global_load_dword %0, %7, %8 sc1\n\ts_waitcnt vmcnt(0)"
               : "=&v"(v) : "v"(a[0]), "v"(a[1]), "v"(a[2]), "v"(a[3]), "v"(a[4]), "v"(a[5]), "v"(a[6]), "s"(ws) : "memory");
}
#define BAR() do { asm volatile("s_waitcnt lgkmcnt(0)" ::: "memory"); __builtin_amdgcn_s_barrier(); asm volatile("" ::: "memory"); } while (0)
__device__ __forceinline__ void dma8k(const char* ws, unsigned voff, unsigned lds) {
  unsigned keep;
  lds = __builtin_amdgcn_readfirstlane(lds + 4096);
  asm volatile("s_mov_b32 %0, m0\n\ts_mov_b32 m0, %3\n\ts_nop 0\n\t"
               "global_load_lds_dwordx4 %1, %2 offset:-4096 sc1\n\tglobal_load_lds_dwordx4 %1, %2 offset:-3072 sc1\n\t"
               "global_load_lds_dwordx4 %1, %2 offset:-2048 sc1\n\tglobal_load_lds_dwordx4 %1, %2 offset:-1024 sc1\n\t"
               "global_load_lds_dwordx4 %1, %2 sc1\n\tglobal_load_lds_dwordx4 %1, %2 offset:1024 sc1\n\t"
               "global_load_lds_dwordx4 %1, %2 offset:2048 sc1\n\tglobal_load_lds_dwordx4 %1, %2 offset:3072 sc1\n\t"
               "s_mov_b32 m0, %0"
               : "=&s"(keep) : "v"(voff + 4096), "s"(ws), "s"(lds) : "memory");
}
__device__ __forceinline__ void dma4k(const char* ws, unsigned voff, unsigned lds) {
  unsigned keep;
  lds = __builtin_amdgcn_readfirstlane(lds);
  asm volatile("s_mov_b32 %0, m0\n\ts_mov_b32 m0, %3\n\ts_nop 0\n\t"
               "global_load_lds_dwordx4 %1, %2 sc1\n\tglobal_load_lds_dwordx4 %1, %2 offset:1024 sc1\n\t"
               "global_load_lds_dwordx4 %1, %2 offset:2048 sc1\n\tglobal_load_lds_dwordx4 %1, %2 offset:3072 sc1\n\t"
               "s_mov_b32 m0, %0"
               : "=&s"(keep) : "v"(voff), "s"(ws), "s"(lds) : "memory");
}
__device__ __forceinline__ void dma1k(const char* ws, unsigned voff, unsigned lds) {
  unsigned keep;
  lds = __builtin_amdgcn_readfirstlane(lds);
  asm volatile("s_mov_b32 %0, m0\n\ts_mov_b32 m0, %3\n\ts_nop 0\n\tglobal_load_lds_dwordx4 %1, %2 sc1\n\ts_mov_b32 m0, %0"
               : "=&s"(keep) : "v"(voff), "s"(ws), "s"(lds) : "memory");
}

constexpr float GC1 = -2.f * 0.7978845608028654f * 1.4426950408889634f, GC2 = GC1 * 0.044715f;
__device__ __forceinline__ u32x4 pair16(u32x2 a, u32x2 b) {
  auto rx = __builtin_amdgcn_permlane32_swap(a.x, b.x, false, false);
  auto ry = __builtin_amdgcn_permlane32_swap(a.y, b.y, false, false);
  return u32x4{rx[0], ry[0], rx[1], ry[1]};
}
__device__ __forceinline__ float allg_max(float m) {
  auto r = __builtin_amdgcn_permlane32_swap(f2u(m), f2u(m), false, false);
  m = fmaxf(u2f(r[0]), u2f(r[1]));
  auto q = __builtin_amdgcn_permlane16_swap(f2u(m), f2u(m), false, false);
  return fmaxf(u2f(q[0]), u2f(q[1]));
}

__device__ __forceinline__ void dma_x(char* ws, unsigned smb, int xi, int T0, int X, int lane) {
  const unsigned src = OFF_X + xi * X_BYTES + (unsigned)T0 * 14 * KB + lane * 16;
#pragma unroll
  for (int k = 0; k < 7; ++k) dma8k(ws, src + k * 8192, smb + X * L_REG + k * 8192);
}
__device__ __forceinline__ void stat_loads(char* ws, u32x4 (&p)[14], int xi, int T0, int lane) {
  const unsigned vo = (lane >> 4) * 3584 + (lane & 15) * 16 + OFF_ST + xi * ST_BYTES + (unsigned)T0 * 3584;
  asm volatile("global_load_dwordx4 %0, %14, %15 sc1\n\tglobal_load_dwordx4 %1, %14, %15 offset:256 sc1\n\t"
               "global_load_dwordx4 %2, %14, %15 offset:512 sc1\n\tglobal_load_dwordx4 %3, %14, %15 offset:768 sc1\n\t"
               "global_load_dwordx4 %4, %14, %15 offset:1024 sc1\n\tglobal_load_dwordx4 %5, %14, %15 offset:1280 sc1\n\t"
               "global_load_dwordx4 %6, %14, %15 offset:1536 sc1\n\tglobal_load_dwordx4 %7, %14, %15 offset:1792 sc1\n\t"
               "global_load_dwordx4 %8, %14, %15 offset:2048 sc1\n\tglobal_load_dwordx4 %9, %14, %15 offset:2304 sc1\n\t"
               "global_load_dwordx4 %10, %14, %15 offset:2560 sc1\n\tglobal_load_dwordx4 %11, %14, %15 offset:2816 sc1\n\t"
               "global_load_dwordx4 %12, %14, %15 offset:3072 sc1\n\tglobal_load_dwordx4 %13, %14, %15 offset:3328 sc1\n\t"
               "s_waitcnt vmcnt(14)"
               : "=&v"(p[0]), "=&v"(p[1]), "=&v"(p[2]), "=&v"(p[3]), "=&v"(p[4]), "=&v"(p[5]), "=&v"(p[6]), "=&v"(p[7]), "=&v"(p[8]),
                 "=&v"(p[9]), "=&v"(p[10]), "=&v"(p[11]), "=&v"(p[12]), "=&v"(p[13])
               : "v"(vo), "s"(ws) : "memory");
}
__device__ __forceinline__ bool stat_wait_poll(char* ws, u32x4 (&p)[14], unsigned off, unsigned tgt) {
  unsigned v;
  asm volatile("global_load_dword %0, %15, %16 sc1\n\ts_waitcnt vmcnt(0)"
               : "=&v"(v), "+v"(p[0]), "+v"(p[1]), "+v"(p[2]), "+v"(p[3]), "+v"(p[4]), "+v"(p[5]), "+v"(p[6]), "+v"(p[7]), "+v"(p[8]), "+v"(p[9]),
                 "+v"(p[10]), "+v"(p[11]), "+v"(p[12]), "+v"(p[13]) : "v"(off), "s"(ws) : "memory");
  return __all(v >= tgt);
}
__device__ __forceinline__ void stat_finish(char* sm, lflag fl, const u32x4 (&p)[14], int X, unsigned stage, int lane) {
  float a[4];
#pragma unroll
  for (int r = 0; r < 4; ++r) {
    float t0 = u2f(p[0][r]) + u2f(p[1][r]), t1 = u2f(p[2][r]) + u2f(p[3][r]);
    OPQ(t0); OPQ(t1);
#pragma unroll
    for (int k = 4; k < 14; k += 2) { t0 += u2f(p[k][r]); t1 += u2f(p[k + 1][r]); OPQ(t0); OPQ(t1); }
    a[r] = t0 + t1;
  }
  float ms[4], rsd[4];
#pragma unroll
  for (int r = 0; r < 4; ++r) {
    float s1 = a[r] + u2f(__builtin_amdgcn_update_dpp(0u, f2u(a[r]), 0x128, 0xf, 0xf, false));
    float s2 = u2f(__builtin_amdgcn_update_dpp(0u, f2u(s1), 0x124, 0xf, 0xf, false));
    float mean = s1 * (1.f / D_);
    OPQ(mean);
    float var = fmaxf(__builtin_fmaf(-mean, mean, s2 * (1.f / D_)), 0.f);
    ms[r] = mean; rsd[r] = __builtin_amdgcn_rsqf(var + 1e-5f);
  }
  if ((lane & 15) < 4) {
    char* d = sm + L_ST + X * 512 + ((lane >> 4) * 16 + (lane & 3) * 4) * 8;
    *(f32x4*)d = f32x4{ms[0], rsd[0], ms[1], rsd[1]};
    *(f32x4*)(d + 16) = f32x4{ms[2], rsd[2], ms[3], rsd[3]};
  }
  asm volatile("" ::: "memory");
  fl[F_SRDY + X] = stage;
}

__device__ __forceinline__ void serve_x(char* ws, char* sm, unsigned smb, lflag fl, int xi, int c, unsigned cnt, unsigned tgt, int lfA, int lfB,
                                        unsigned ltgt, unsigned stage, int lane) {
  u32x4 p[14];
  lwait(fl, lfA, ltgt);
  gpoll(ws, cnt, cnt + 128, tgt);
  dma_x(ws, smb, xi, 8 * c, 0, lane);
  stat_loads(ws, p, xi, 8 * c, lane);
  fl[F_RDY] = stage;
  const bool now = stat_wait_poll(ws, p, cnt + (2 + (lane & 1)) * 128, tgt) && lready(fl, lfB, ltgt);
  if (now) dma_x(ws, smb, xi, 8 * c + 4, 1, lane);
  stat_finish(sm, fl, p, 0, stage, lane);
  if (!now) {
    lwait(fl, lfB, ltgt);
    gpoll(ws, cnt + 256, cnt + 384, tgt);
    dma_x(ws, smb, xi, 8 * c + 4, 1, lane);
  }
  stat_loads(ws, p, xi, 8 * c + 4, lane);
  fl[F_RDY + 1] = stage;
  (void)stat_wait_poll(ws, p, cnt, 0u);
  stat_finish(sm, fl, p, 1, stage, lane);
}

template <class PRE>
__device__ __forceinline__ u32x4 attn_tile(const char* kv, u32x4 q, int qt, int lane, PRE pre) {
  const int i = lane & 15, g = lane >> 4;
  const float NEG = -__builtin_inff();
  f32x4 s[18];
  u32x4 kf[18];
#pragma unroll
  for (int j = 0; j < 6; ++j) kf[j] = lds16(kv + (j >> 1) * 4096 + (j & 1) * 1024);
#pragma unroll
  for (int j = 0; j < 18; ++j) {
    if (j + 6 < 18) kf[j + 6] = lds16(kv + ((j + 6) >> 1) * 4096 + (j & 1) * 1024);
    SCHED();
    s[j] = mfma(kf[j], q, f32x4{0.f, 0.f, 0.f, 0.f});
    if (j % 2 == 1) pre(j / 2);
    SCHED();
  }
  u32x4 vf[18];
#pragma unroll
  for (int j = 0; j < 4; ++j) vf[j] = lds16(kv + (j >> 1) * 4096 + 2048 + (j & 1) * 1024);
  const int dq = 16 * (qt & 1) + i - 4 * g;
#pragma unroll
  for (int u = 0; u < 2; ++u)
#pragma unroll
    for (int r = 0; r < 4; ++r) {
      const bool lo = dq < 16 * u + r;
      s[u][r] = lo ? s[u][r] : NEG;
      s[16 + u][r] = lo ? NEG : s[16 + u][r];
    }
  float mx[4];
#pragma unroll
  for (int r = 0; r < 4; ++r) mx[r] = fmaxf(s[2][r], s[3][r]);
#pragma unroll
  for (int j = 4; j < 20; j += 2)
#pragma unroll
    for (int r = 0; r < 4; ++r) mx[r] = fmaxf(fmaxf(mx[r], s[j % 18][r]), s[(j + 1) % 18][r]);
  const float m = allg_max(fmaxf(fmaxf(mx[0], mx[1]), fmaxf(mx[2], mx[3])));
  constexpr float SC = 0.18898223650461361f * 1.4426950408889634f;
  const float mc = -m * SC;
  const f32x4 m4 = {mc, mc, mc, mc}, c4 = {SC, SC, SC, SC};
  SCHED();
#pragma unroll
  for (int j = 0; j < 18; ++j) s[j] = __builtin_elementwise_fma(s[j], c4, m4);
  pre(9);
  SCHED();
  f32x4 o0 = {0.f, 0.f, 0.f, 0.f}, o1 = {0.f, 0.f, 0.f, 0.f};
#pragma unroll
  for (int j = 0; j < 9; ++j) {
    if (j + 2 < 9) { vf[2 * j + 4] = lds16(kv + (j + 2) * 4096 + 2048); vf[2 * j + 5] = lds16(kv + (j + 2) * 4096 + 3072); }
    float p[8];
#pragma unroll
    for (int k = 0; k < 8; ++k) p[k] = __builtin_amdgcn_exp2f(s[2 * j + (k >> 2)][k & 3]);
    const u32x4 pf = {pk(p[0], p[1]), pk(p[2], p[3]), pk(p[4], p[5]), pk(p[6], p[7])};
    o0 = mfma(vf[2 * j], pf, o0);
    o1 = mfma(vf[2 * j + 1], pf, o1);
    if (j < 6) { pre(10 + 2 * j); pre(11 + 2 * j); }
    SCHED();
  }
  auto r = __builtin_amdgcn_permlane32_swap(f2u(o1[0]), f2u(o1[0]), false, false);
  auto t = __builtin_amdgcn_permlane16_swap(r[1], r[1], false, false);
  const float inv = __builtin_amdgcn_rcpf(u2f(t[1]));
  return u32x4{pk(o0[0] * inv, o0[1] * inv), pk(o0[2] * inv, o0[3] * inv), pk(o1[0] * inv, o1[1] * inv), pk(o1[2] * inv, o1[3] * inv)};
}

__device__ __forceinline__ u32x4 geglu8(const f32x16& acc, f32x2 ms, const f32x4& cg0, const f32x4& cg1, const f32x4& cu0, const f32x4& cu1) {
  const float q = ms.y * ms.y, k1 = GC1 * ms.y, k2 = GC2 * ms.y * q, ir = __builtin_amdgcn_rcpf(q), nm = -ms.x;
  float a[8], b[8], t[8];
#pragma unroll
  for (int r = 0; r < 8; ++r) { a[r] = __builtin_fmaf(nm, r < 4 ? cg0[r & 3] : cg1[r & 3], acc[r]); OPQ(a[r]); }
  SCHED();
#pragma unroll
  for (int r = 0; r < 8; ++r) { t[r] = a[r] * a[r]; OPQ(t[r]); }
  SCHED();
#pragma unroll
  for (int r = 0; r < 8; ++r) { t[r] = __builtin_fmaf(t[r], k2, k1); OPQ(t[r]); }
  SCHED();
#pragma unroll
  for (int r = 0; r < 8; ++r) { t[r] = t[r] * a[r]; OPQ(t[r]); }
  SCHED();
#pragma unroll
  for (int r = 0; r < 8; ++r) {
    t[r] = __builtin_amdgcn_exp2f(t[r]);
    b[r] = __builtin_fmaf(nm, r < 4 ? cu0[r & 3] : cu1[r & 3], acc[r + 8]); OPQ(b[r]);
  }
  SCHED();
#pragma unroll
  for (int r = 0; r < 8; ++r) { t[r] = __builtin_fmaf(t[r], ir, ir); OPQ(t[r]); }
  SCHED();
#pragma unroll
  for (int r = 0; r < 8; ++r) { t[r] = __builtin_amdgcn_rcpf(t[r]); b[r] = a[r] * b[r]; OPQ(b[r]); }
  SCHED();
#pragma unroll
  for (int r = 0; r < 8; ++r) { t[r] = t[r] * b[r]; OPQ(t[r]); }
  SCHED();
  return u32x4{pks(t[0], t[1]), pks(t[2], t[3]), pks(t[4], t[5]), pks(t[6], t[7])};
}

__device__ __forceinline__ void fin5(char* ws, uint16_t* out, char* sm, int X, int nt, int P, int J, int l, int lane) {
  const int i = lane & 15, g = lane >> 4;
  const u32x4 xr = lds16(sm + L_RES + X * 4096 + nt * 1024 + lane * 16);
  const char* red = sm + X * L_REG + nt * 2048 + lane * 16;
  f32x4 pa[7], pb[7];
#pragma unroll
  for (int k = 0; k < 7; ++k) { pa[k] = *(const f32x4*)(red + k * 8192); pb[k] = *(const f32x4*)(red + k * 8192 + 1024); }
  float v[8] = {bflo(xr.x), bfhi(xr.x), bflo(xr.y), bfhi(xr.y), bflo(xr.z), bfhi(xr.z), bflo(xr.w), bfhi(xr.w)};
  SCHED();
#pragma unroll
  for (int k = 0; k < 7; ++k) {
#pragma unroll
    for (int r = 0; r < 8; ++r) { v[r] += r < 4 ? pa[k][r] : pb[k][r - 4]; OPQ(v[r]); }
    SCHED();
  }
  const u32x4 o = {pks(v[0], v[1]), pks(v[2], v[3]), pks(v[4], v[5]), pks(v[6], v[7])};
  if (l == 0) {
    sth(o, ws, (g & 1) * 512 + (i + 16 * (g >> 1)) * 16, OFF_X + 2 * X_BYTES + (P * 28 + J) * KB);
    float s1 = v[0] + v[1], s2 = v[0] * v[0], s3 = v[2] + v[3], s4 = v[1] * v[1];
    OPQ(s1); OPQ(s2); OPQ(s3); OPQ(s4);
    s1 += v[4]; s3 += v[5]; s2 = __builtin_fmaf(v[2], v[2], s2); s4 = __builtin_fmaf(v[3], v[3], s4);
    OPQ(s1); OPQ(s2); OPQ(s3); OPQ(s4);
    s1 += v[6]; s3 += v[7]; s2 = __builtin_fmaf(v[4], v[4], s2); s4 = __builtin_fmaf(v[5], v[5], s4);
    OPQ(s1); OPQ(s2); OPQ(s3); OPQ(s4);
    s1 += s3; s2 = __builtin_fmaf(v[6], v[6], s2); s4 = __builtin_fmaf(v[7], v[7], s4);
    OPQ(s1); OPQ(s2); OPQ(s4);
    s2 += s4;
    auto r = __builtin_amdgcn_permlane32_swap(f2u(s1), f2u(s2), false, false);
    const float sv = u2f(r[0]) + u2f(r[1]);
    sth32(f2u(sv), ws, (g & 1) * 3584 + (lane >> 5) * 64 + i * 4, OFF_ST + 2 * ST_BYTES + (2 * P * 28 + J) * 128);
  } else {
    uint16_t* op = out + (size_t)(P * 32 + (lane & 31)) * D_ + 16 * J + 4 * (lane >> 5);
    *(u32x2*)op = u32x2{o.x, o.y};
    *(u32x2*)(op + 8) = u32x2{o.z, o.w};
  }
}

#define IDS()                                                                                                   \
  const int lane = opaque_v(lane0), wave = opaque(wave0), b = opaque(b0), xcd = b & 7, rk = b >> 3;             \
  const int c1 = MAP1 == 8 ? 2 * xcd + (rk & 1) : MAP1 == 4 ? 4 * (xcd >> 1) + (rk & 3) : 8 * (xcd >> 2) + (rk & 7); \
  const int h1 = MAP1 == 8 ? rk >> 1 : MAP1 == 4 ? 8 * (xcd & 1) + (rk >> 2) : 4 * (xcd & 3) + (rk >> 3);       \
  const int c3 = MAP3 == 8 ? 2 * xcd + (rk & 1) : 4 * (xcd >> 1) + (rk & 3);                                    \
  const int u5 = MAP3 == 8 ? rk >> 1 : 8 * (xcd & 1) + (rk >> 2);                                               \
  const int m3 = (u5 & 3) * 4 + (u5 >> 2);                                                                      \
  const bool act5 = u5 < 14;                                                                                    \
  const int th = u5 & 1, ng = u5 >> 1, i = lane & 15, g = lane >> 4;                                            \
  const int part = wave < 6 ? 2 - (wave >> 1) : 0, nt1 = wave & 1, nt6 = 2 * part + nt1, w7 = min(wave, 6), J4 = 7 * h1 + w7, J3 = 7 * (m3 & 3) + w7; \
  const unsigned l16 = lane * 16, half_off = (lane & 31) * 16, res_off = (i + 16 * (g & 1)) * 16 + 8 * (g >> 1), f32_off = (g & 1) * 512 + (i + 16 * (g >> 1)) * 16; \
  (void)c1; (void)h1; (void)c3; (void)act5; (void)ng; (void)th; (void)part; (void)nt1; (void)J4; (void)J3; (void)l16; (void)half_off; (void)res_off; (void)f32_off; (void)nt6
__global__ __launch_bounds__(NT, 2) void k_main(char* ws, uint16_t* out) {
  extern __shared__ __attribute__((aligned(16))) char sm[];
  const int tid = threadIdx.x, lane0 = tid & 63, wave0 = __builtin_amdgcn_readfirstlane(tid >> 6), b0 = blockIdx.x;
  IDS();
  lflag fl = (lflag)(LAS char*)(sm + L_FL);
  const rsrc_t rs = __builtin_amdgcn_make_buffer_rsrc(ws, 0, WS_USED, 0x00020000);
  int stn = 0; (void)stn;
  const unsigned smb = (unsigned)(uintptr_t)(LAS char*)sm;
  u32x4 w1[14];
  if (wave0 == 7) {
    PRIO(3);
    const unsigned src = OFF_X + 4 * X_BYTES + c1 * 112 * KB + l16;
#pragma unroll
    for (int k = 0; k < 7; ++k) dma8k(ws, src + k * 8192, smb + k * 8192);
    dma1k(ws, OFF_CS + h1 * 2048 + l16, smb + L_CS);
    dma1k(ws, OFF_CS + h1 * 2048 + 1024 + l16, smb + L_CS + 1024);
    fl[lane] = 0u;
    *(f32x2*)(sm + L_ST + lane * 8) = f32x2{0.f, 1.f};
    *(f32x2*)(sm + L_ST + 512 + lane * 8) = f32x2{0.f, 1.f};
    undef<14>(w1);
  } else if (wave0 < 6) {
#pragma unroll
    for (int s = 0; s < 14; ++s) w1[s] = ldwf(rs, l16, OFF_WQKV + (h1 * 6 + nt6) * 14 * KB, s);
  } else {
    undef<14>(w1);
  }
  BAR();

#pragma unroll 1
  for (int l = 0; l < 2; ++l) {
    const unsigned st = 5 * l;
    u32x4 w3[16], w4[28], w5[32];
#define PRE1(k)                                                                                         \
  do {                                                                                                  \
    if ((k) < 16) w3[(k)] = ldwf(rs, l16, OFF_WO + J3 * 16 * KB, (k));                                  \
    else if ((k) < 44) w4[(k) - 16] = ldwf(rs, l16, OFF_WGU + J4 * 28 * KB, (k) - 16);                  \
    else w5[(k) - 44] = ldwf(rs, l16, OFF_WD + ((act5 ? ng : 0) * 112 + 16 * w7) * 2 * KB, (k) - 44);   \
  } while (0)
#define PRE34(k0, k1)                                                                                   \
  SCHED();                                                                                              \
  _Pragma("unroll") for (int k = k0; k < k1; ++k) PRE1(k);                                              \
  SCHED();
    if (wave0 == 7) {
      IDS();
      if (l == 0) {
        const unsigned src = OFF_X + 4 * X_BYTES + (c1 * 112 + 56) * KB + l16;
#pragma unroll
        for (int k = 0; k < 7; ++k) {
          dma8k(ws, src + k * 8192, smb + L_REG + k * 8192);
          if (k == 3) { WAIT_VM(32); fl[F_RDY] = 1u; fl[F_SRDY] = 1u; }
        }
        WAIT_VM(0);
        fl[F_RDY + 1] = 1u; fl[F_SRDY + 1] = 1u;
      } else {
        serve_x(ws, sm, smb, fl, 2, c1, CNT_X2 + 4 * c1 * 128, 7, F_ARR4, (act5 ? F_ARR5 : F_ARR4) + 1, act5 ? 4 : 7, st + 1, lane);
        STAMP();
      }
      lwait(fl, F_ARR1, 6 * (l + 1));
#pragma unroll 1
      for (int k = 0; k < 8 - 4 * c1; ++k) {
        const u32x4 kz = {0u, 0u, g == 3 ? 0xff80u : 0u, 0u}, vz = {0u, 0u, 0u, 0u};
        char* d = sm + L_HALO + k * 4096 + l16;
        *(u32x4*)d = kz; *(u32x4*)(d + 1024) = kz; *(u32x4*)(d + 2048) = vz; *(u32x4*)(d + 3072) = vz;
      }
      if (c1 > 0) gpoll(ws, CNT_KV + ((l * 16 + h1) * 16 + max(c1 - 2, 0)) * 128, CNT_KV + ((l * 16 + h1) * 16 + c1 - 1) * 128, 4);
      STAMP();
      const unsigned src = OFF_KV + l * KV_BYTES + (h1 * 64 + 4 * c1 - 8) * 4096 + l16;
#pragma unroll
      for (int k = 0; k < 4; ++k) if (2 * c1 + k >= 4) dma8k(ws, src + k * 8192, smb + L_HALO + k * 8192);
      WAIT_VM(0);
      PRIO(0);
      undef<6>(w3);
    } else {
      if (wave0 < 6) touch<14>(w1);
      auto half1 = [&](auto x_t) {
        constexpr int X = decltype(x_t)::value;
        IDS();
        if (wave < 6) {
          lwait(fl, F_RDY + X, st + 1);
          STAMP();
          const char* xs = sm + X * L_REG + (g >> 1) * 1024 + half_off;
          const char* stp = sm + L_ST + X * 512;
          const unsigned so_kv = OFF_KV + l * KV_BYTES + (h1 * 64 + 4 * c1 + 2 * X) * 4096;
          f32x4 acc[4];
          u32x4 xf[14][4];
          unsigned sfl;
#pragma unroll
          for (int t = 0; t < 4; ++t) acc[t] = f32x4{0.f, 0.f, 0.f, 0.f};
#define P1_X(s, t) lds16(xs + ((t) >> 1) * 28 * 1024 + (s) * 2048 + ((t) & 1) * 512)
#define P1_LOOP(MM)                                                                                     \
  _Pragma("unroll") for (int s = 0; s < 2; ++s) {                                                       \
    _Pragma("unroll") for (int t = 0; t < 4; ++t) xf[s][t] = P1_X(s, t);                                \
  }                                                                                                     \
  _Pragma("unroll") for (int s = 0; s < 14; ++s) {                                                      \
    if (s + 2 < 14) { _Pragma("unroll") for (int t = 0; t < 4; ++t) xf[s + 2][t] = P1_X(s + 2, t); }    \
    if (s == 12) { sfl = fl[F_SRDY + X]; asm volatile("" ::: "memory"); P1_EPI_LOADS }                  \
    SCHED();                                                                                            \
    _Pragma("unroll") for (int t = 0; t < 4; ++t) acc[t] = MM;                                          \
    if (X == 1 && s >= 2 && s < 8) PRE1(s - 2);                                                         \
    SCHED();                                                                                            \
  }                                                                                                     \
  if ((unsigned)__builtin_amdgcn_readfirstlane(sfl) < st + 1) { lwait(fl, F_SRDY + X, st + 1); P1_EPI_LOADS }
          if (part < 2) {
            f32x4 cs1; f32x2 m[4];
#define P1_EPI_LOADS cs1 = *(const f32x4*)(sm + L_CS + (nt6 * 16 + 4 * g) * 4); _Pragma("unroll") for (int t = 0; t < 4; ++t) m[t] = *(const f32x2*)(stp + t * 128 + i * 8);
            P1_LOOP(mfma(w1[s], xf[s][t], acc[t]))
#undef P1_EPI_LOADS
            STAMP();
            PRIO(2);
            float v[16];
#pragma unroll
            for (int k = 0; k < 16; ++k) { v[k] = __builtin_fmaf(-m[k >> 2].x, cs1[k & 3], acc[k >> 2][k & 3]); OPQ(v[k]); }
            SCHED();
#pragma unroll
            for (int k = 0; k < 16; ++k) { v[k] *= m[k >> 2].y; OPQ(v[k]); }
            SCHED();
            if (nt6 == 1 && g == 3) { v[0] = 1.f; v[4] = 1.f; v[8] = 1.f; v[12] = 1.f; }
            const u32x4 o0 = pair16(u32x2{pk(v[0], v[1]), pk(v[2], v[3])}, u32x2{pk(v[4], v[5]), pk(v[6], v[7])});
            const u32x4 o1 = pair16(u32x2{pk(v[8], v[9]), pk(v[10], v[11])}, u32x2{pk(v[12], v[13]), pk(v[14], v[15])});
            const unsigned off = (g >> 1) * 1024 + nt1 * 512 + half_off;
            if (part == 0) {
              *(u32x4*)(sm + L_Q + X * 4096 + off) = o0;
              *(u32x4*)(sm + L_Q + X * 4096 + 2048 + off) = o1;
            } else {
              sth(o0, ws, off, so_kv); sth(o1, ws, off, so_kv + 4096);
              *(u32x4*)(sm + L_OWN + X * 8192 + off) = o0;
              *(u32x4*)(sm + L_OWN + X * 8192 + 4096 + off) = o1;
            }
          } else {
            float c; f32x4 ma[4], mb[4];
#define P1_EPI_LOADS c = *(const float*)(sm + L_CS + (nt6 * 16 + i) * 4); _Pragma("unroll") for (int t = 0; t < 4; ++t) { ma[t] = *(const f32x4*)(stp + t * 128 + g * 32); mb[t] = *(const f32x4*)(stp + t * 128 + g * 32 + 16); }
            P1_LOOP(mfma(xf[s][t], w1[s], acc[t]))
#undef P1_EPI_LOADS
            STAMP();
            PRIO(2);
            float v[16];
#pragma unroll
            for (int k = 0; k < 16; ++k) { const f32x4 mm = (k & 2) ? mb[k >> 2] : ma[k >> 2]; v[k] = __builtin_fmaf(-mm[2 * (k & 1)], c, acc[k >> 2][k & 3]); OPQ(v[k]); }
            SCHED();
#pragma unroll
            for (int k = 0; k < 16; ++k) { const f32x4 mm = (k & 2) ? mb[k >> 2] : ma[k >> 2]; v[k] *= mm[2 * (k & 1) + 1]; OPQ(v[k]); }
            SCHED();
            u32x4 o0 = {pk(v[0], v[1]), pk(v[2], v[3]), pk(v[4], v[5]), pk(v[6], v[7])};
            u32x4 o1 = {pk(v[8], v[9]), pk(v[10], v[11]), pk(v[12], v[13]), pk(v[14], v[15])};
            if (nt1 == 1 && i == 12) { o0 = u32x4{0x3f803f80u, 0x3f803f80u, 0x3f803f80u, 0x3f803f80u}; o1 = o0; }
            const unsigned off = 2048 + nt1 * 1024 + l16;
            sth(o0, ws, off, so_kv); sth(o1, ws, off, so_kv + 4096);
            *(u32x4*)(sm + L_OWN + X * 8192 + off) = o0;
            *(u32x4*)(sm + L_OWN + X * 8192 + 4096 + off) = o1;
          }
          if (X == 0) arrive_nr(fl, F_ARR1);
          STAMP();
          if (X == 1 && wave < 4) {
            WAIT_VM(0);
            gadd(ws, CNT_KV + ((l * 16 + h1) * 16 + c1) * 128);
            STAMP();
          }
        } else if (X == 1) {
          lwait(fl, F_SRDY + X, st + 1);
          PRE34(0, 6)
        }
        PRIO(0);
      };
      half1(std::integral_constant<int, 0>{});
      half1(std::integral_constant<int, 1>{});
    }
    BAR();
    {
      IDS();
      STAMP();
      const int T = 8 * c1 + wave;
      const u32x4 q = lds16(sm + L_Q + wave * 1024 + l16);
      const char* kv = sm + L_HALO + (wave >> 1) * 4096 + l16;
      const u32x4 o = attn_tile(kv, q, wave, lane, [&](int k) { PRE1(6 + k); });
      sth(o, ws, l16, OFF_O + l * O_BYTES + (T * 16 + h1) * KB);
      STAMP();
      WAIT_VM(0);
      gadd(ws, CNT_O + (l * 128 + T) * 128);
      STAMP();
      arrive_nr(fl, F_GO2);
      if (wave < 7) { lwait(fl, F_GO2, 8 * (l + 1)); PRE34(28, 36) } else { undef<8>(&w4[12]); }
      if (wave == 7) {
        PRIO(3);
        const int P = 4 * c3 + (m3 >> 2);
        gpoll(ws, CNT_O + (l * 128 + 2 * P) * 128, CNT_O + (l * 128 + 2 * P + 1) * 128, 16);
        STAMP();
        const unsigned src = OFF_O + l * O_BYTES + P * 32 * KB + l16;
#pragma unroll
        for (int k = 0; k < 4; ++k) dma8k(ws, src + k * 8192, smb + L_REG + k * 8192);
        dma8k(ws, OFF_X + 2 * l * X_BYTES + (P * 28 + 7 * (m3 & 3)) * KB + l16, smb + L_RES);
        WAIT_VM(0);
      }
    }
    BAR();
    if (wave0 == 7) {
      IDS();
      serve_x(ws, sm, smb, fl, 2 * l + 1, c1, CNT_X1 + (l * 64 + 4 * c1) * 128, 4, F_ARR3, F_ARR3, 7 * (l + 1), st + 4, lane);
      STAMP();
      if (act5) {
        const unsigned cH = CNT_H + (l * 16 + c3) * 2 * 128;
        const int P = 4 * c3 + th;
        const unsigned rsrc = OFF_X + (2 * l + 1) * X_BYTES + (P * 28 + 4 * ng) * KB + f32_off;
        lwait(fl, F_ARR4 + 1, 7 * l + 4);
        gpoll(ws, cH, cH, 16);
        fl[F_RDY] = st + 5;
        lwait(fl, F_GO4, l + 1);
        STAMP();
        __builtin_amdgcn_s_sleep(8);
        gpoll(ws, cH + 128, cH + 128, 16);
        fl[F_RDY + 1] = st + 5;
        STAMP();
        dma4k(ws, rsrc, smb + L_RES);
        dma4k(ws, rsrc + 56 * KB, smb + L_RES + 4096);
        WAIT_VM(0);
        arrive_nr(fl, F_RED + 1);
        lwait(fl, F_RED, 7 * (l + 1));
#pragma unroll 1
        for (int nt = 0; nt < 4; ++nt) fin5(ws, out, sm, 0, nt, P, 4 * ng + nt, l, lane);
        if (l == 0) { WAIT_VM(0); gadd(ws, CNT_X2 + P * 128); }
        STAMP();
      }
      undef<14>(w1);
    } else {
      {
        IDS();
        STAMP();
        const char* os = sm + L_REG + l16;
        u32x4 oa[16], ob[16];
#pragma unroll
        for (int s = 0; s < 6; ++s) { oa[s] = lds16(os + s * 1024); ob[s] = lds16(os + (16 + s) * 1024); }
        const u32x2 xa = *(const u32x2*)(sm + L_RES + wave * 1024 + res_off), xb = *(const u32x2*)(sm + L_RES + wave * 1024 + 512 + res_off);
        f32x4 a0 = {0.f, 0.f, 0.f, 0.f}, a1 = {0.f, 0.f, 0.f, 0.f};
#pragma unroll
        for (int s = 0; s < 16; ++s) {
          SCHED();
          a0 = mfma(w3[s], oa[s], a0); a1 = mfma(w3[s], ob[s], a1);
          if (s < 8) PRE1(36 + s);
          SCHED();
          if (s + 6 < 16) { oa[s + 6] = lds16(os + (s + 6) * 1024); ob[s + 6] = lds16(os + (22 + s) * 1024); }
        }
        PRIO(2);
        float v[8] = {a0[0] + bflo(xa.x), a0[1] + bfhi(xa.x), a0[2] + bflo(xa.y), a0[3] + bfhi(xa.y),
                      a1[0] + bflo(xb.x), a1[1] + bfhi(xb.x), a1[2] + bflo(xb.y), a1[3] + bfhi(xb.y)};
#pragma unroll
        for (int k = 0; k < 8; ++k) OPQ(v[k]);
        const int P = 4 * c3 + (m3 >> 2);
        sth(pair16(u32x2{pk(v[0], v[1]), pk(v[2], v[3])}, u32x2{pk(v[4], v[5]), pk(v[6], v[7])}), ws, l16,
            OFF_X + (2 * l + 1) * X_BYTES + (P * 28 + J3) * KB);
        float s1a = v[0] + v[1], s1b = v[4] + v[5], s2a = v[0] * v[0], s2b = v[4] * v[4];
        OPQ(s1a); OPQ(s1b); OPQ(s2a); OPQ(s2b);
        s1a += v[2]; s1b += v[6]; s2a = __builtin_fmaf(v[1], v[1], s2a); s2b = __builtin_fmaf(v[5], v[5], s2b);
        OPQ(s1a); OPQ(s1b); OPQ(s2a); OPQ(s2b);
        s1a += v[3]; s1b += v[7]; s2a = __builtin_fmaf(v[2], v[2], s2a); s2b = __builtin_fmaf(v[6], v[6], s2b);
        OPQ(s2a); OPQ(s2b);
        s2a = __builtin_fmaf(v[3], v[3], s2a); s2b = __builtin_fmaf(v[7], v[7], s2b);
        auto r1 = __builtin_amdgcn_permlane32_swap(f2u(s1a), f2u(s1b), false, false);
        auto r2 = __builtin_amdgcn_permlane32_swap(f2u(s2a), f2u(s2b), false, false);
        float s1 = u2f(r1[0]) + u2f(r1[1]), s2 = u2f(r2[0]) + u2f(r2[1]);
        OPQ(s1); OPQ(s2);
        auto t = __builtin_amdgcn_permlane16_swap(f2u(s1), f2u(s2), false, false);
        sth32(f2u(u2f(t[0]) + u2f(t[1])), ws, (g >> 1) * 3584 + (lane & 31) * 4, OFF_ST + (2 * l + 1) * ST_BYTES + (2 * P * 28 + J3) * 128);
        STAMP();
        WAIT_VM(0);
        if (arrive(fl, F_ARR3, 7 * (l + 1))) { gadd(ws, CNT_X1 + (l * 64 + P) * 128); arrive_nr(fl, F_GO3); }
        lwait(fl, F_GO3, l + 1);
        PRE34(44, 52)

        PRIO(0);
        STAMP();
      }
      u32x4 hf[2][16];
      auto half4 = [&](auto x_t) {
        constexpr int X = decltype(x_t)::value;
        IDS();
        const char* xs = sm + X * L_REG + f32_off;
        const char* stp = sm + L_ST + X * 512 + (lane & 31) * 8;
        const char* csp = sm + L_CS + (96 + wave * 16 + 4 * (lane >> 5)) * 4;
        f32x16 a0, a1;
        u32x4 xa[28], xb[28];
        f32x2 m0, m1; f32x4 cg0, cg1, cu0, cu1;
        unsigned sfl;
        lwait(fl, F_RDY + X, st + 4);
        STAMP();
        PRIO(1 - X);
#define P4_LOOP(STREAM)                                                                                 \
  _Pragma("unroll") for (int r = 0; r < 16; ++r) { a0[r] = 0.f; a1[r] = 0.f; }                          \
  _Pragma("unroll") for (int s = 0; s < 3; ++s) { xa[s] = lds16(xs + s * 1024); xb[s] = lds16(xs + (28 + s) * 1024); } \
  _Pragma("unroll") for (int s = 0; s < 28; ++s) {                                                      \
    if (s + 3 < 28) { xa[s + 3] = lds16(xs + (s + 3) * 1024); xb[s + 3] = lds16(xs + (31 + s) * 1024); } \
    if (s == 25) { sfl = fl[F_SRDY + X]; asm volatile("" ::: "memory"); m0 = *(const f32x2*)stp; m1 = *(const f32x2*)(stp + 256); } \
    if (s == 26) { cg0 = *(const f32x4*)csp; cg1 = *(const f32x4*)(csp + 32); }                         \
    if (s == 27) { cu0 = *(const f32x4*)(csp + 448); cu1 = *(const f32x4*)(csp + 480); }                \
    if (X == 0 && s >= 16 && (s & 1) == 0) PRE1(44 + (s >> 1));                                         \
    SCHED();                                                                                            \
    a0 = mfma32(w4[s], xa[s], a0); a1 = mfma32(w4[s], xb[s], a1);                                       \
    SCHED();                                                                                            \
    if (X == 1 && s == 4 && wave < 4) {                                                                 \
      WAIT_VM(0);                                                                                       \
      if (arrive(fl, F_ARR4, 7 * (l + 1))) gadd(ws, CNT_H + ((l * 16 + c1) * 2) * 128);                 \
    }                                                                                                   \
    if (X == 1 && s >= 5 && s < 23) w5[9 + s] = ldwf(rs, vo5, so5, 9 + s);                              \
    SCHED();                                                                                            \
  }
        const unsigned so5 = OFF_WD + ((act5 ? ng : 0) * 112 + 16 * wave) * 2 * KB, vo5 = opaque_v(l16 + (wave < 4 ? 0u : 0x40000000u));
        P4_LOOP(0)
        if ((unsigned)__builtin_amdgcn_readfirstlane(sfl) < st + 4) {
          lwait(fl, F_SRDY + X, st + 4);
          m0 = *(const f32x2*)stp; m1 = *(const f32x2*)(stp + 256);
        }
        STAMP();
        PRIO(2);
        const unsigned so = OFF_H + l * H_BYTES + ((4 * c1 + 2 * X) * 112 + J4) * KB;
        sth(geglu8(a0, m0, cg0, cg1, cu0, cu1), ws, f32_off, so);
        sth(geglu8(a1, m1, cg0, cg1, cu0, cu1), ws, f32_off, so + 112 * KB);
        STAMP();
        SCHED();
        if (X == 0) {
          if (wave >= 4) {
            WAIT_VM(0);
            if (arrive(fl, F_ARR4, 7 * (l + 1))) gadd(ws, CNT_H + ((l * 16 + c1) * 2) * 128);
          }
        } else {
          WAIT_VM(0);
          if (arrive(fl, F_ARR4 + 1, 7 * (l + 1))) { gadd(ws, CNT_H + ((l * 16 + c1) * 2 + 1) * 128); arrive_nr(fl, F_GO4); }
          lwait(fl, F_GO4, l + 1);
          if (wave >= 4) { PRE34(58, 76) }
          if (act5) lwait(fl, F_RDY + 0, st + 5);
          SCHED();
          const unsigned soh = act5 ? OFF_H + l * H_BYTES + ((4 * c3 + th) * 112 + 16 * wave) * KB : OFF_WD;
#pragma unroll
          for (int s = 0; s < 16; ++s) hf[0][s] = ldhf(rs, f32_off, soh, s);
          SCHED();
          PRIO(0);
          STAMP();
        }
      };
      half4(std::integral_constant<int, 0>{});
      half4(std::integral_constant<int, 1>{});
      SCHED();
      auto mma5 = [&](auto x_t) {
        constexpr int X = decltype(x_t)::value;
        IDS();
        const unsigned soh = OFF_H + l * H_BYTES + ((4 * c3 + 2 + th) * 112 + 16 * wave) * KB;
        f32x16 a0, a1;
#pragma unroll
        for (int r = 0; r < 16; ++r) { a0[r] = 0.f; a1[r] = 0.f; }
#pragma unroll
        for (int s = 0; s < 16; ++s) {
          SCHED();
          a0 = mfma32(w5[2 * s], hf[X][s], a0); a1 = mfma32(w5[2 * s + 1], hf[X][s], a1);
          SCHED();
          if (X == 0) hf[1][s] = ldhf(rs, f32_off, soh, s);
        }
        SCHED();
        char* mine = sm + X * L_REG + wave * 8192 + l16;
#pragma unroll
        for (int k = 0; k < 4; ++k) {
          *(f32x4*)(mine + k * 1024) = f32x4{a0[4 * k], a0[4 * k + 1], a0[4 * k + 2], a0[4 * k + 3]};
          *(f32x4*)(mine + (4 + k) * 1024) = f32x4{a1[4 * k], a1[4 * k + 1], a1[4 * k + 2], a1[4 * k + 3]};
        }
        arrive_nr(fl, F_RED + X);
      };
      if (act5) {
        lwait(fl, F_RDY + 1, st + 5);
        STAMP();
        PRIO(1);
        mma5(std::integral_constant<int, 0>{});
        PRIO(0);
        STAMP();
        mma5(std::integral_constant<int, 1>{});
        STAMP();
      }
      SCHED();
      if (l == 0 && wave0 < 6) {
        IDS();
#pragma unroll
        for (int s = 0; s < 14; ++s) w1[s] = ldwf(rs, l16, OFF_WQKV + (h1 * 6 + nt6) * 14 * KB, s);
      } else {
        undef<14>(w1);
      }
      SCHED();
      if (act5 && wave0 < 4) {
        IDS();
        PRIO(2);
        lwait(fl, F_RED + 1, 8 * (l + 1));
        fin5(ws, out, sm, 1, wave, 4 * c3 + 2 + th, 4 * ng + wave, l, lane);
        STAMP();
        if (l == 0) {
          WAIT_VM(0);
          if (arrive(fl, F_ARR5 + 1, 4)) gadd(ws, CNT_X2 + (4 * c3 + 2 + th) * 128);
        }
        PRIO(0);
        STAMP();
      }
    }
  }
}

extern "C" void kernel_launch(void* const* d_in, const int* in_sizes, int n_in, void* d_out, int out_size, void* d_ws,
                              size_t ws_size, hipStream_t stream) {
  const uint16_t* x = (const uint16_t*)d_in[0];
  const uint16_t* wqkv = (const uint16_t*)d_in[1];
  const uint16_t* wo = (const uint16_t*)d_in[2];
  const uint16_t* wg = (const uint16_t*)d_in[4];
  const uint16_t* wu = (const uint16_t*)d_in[5];
  const uint16_t* wd = (const uint16_t*)d_in[6];
  char* ws = (char*)d_ws;
  (void)hipFuncSetAttribute((const void*)k_main, hipFuncAttributeMaxDynamicSharedMemorySize, L_BYTES);
  k_prep<<<256, NT, 0, stream>>>(x, wqkv, wo, wg, wu, wd, ws);
  k_main<<<256, NT, L_BYTES, stream>>>(ws, (uint16_t*)d_out);
}
```

```cpp
#include <hip/hip_runtime.h>
#include <hip/hip_bf16.h>
#include <stdint.h>
#include <type_traits>

typedef short bf16x8 __attribute__((ext_vector_type(8)));
typedef float f32x4 __attribute__((ext_vector_type(4)));
typedef float f32x2 __attribute__((ext_vector_type(2)));
typedef float f32x16 __attribute__((ext_vector_type(16)));
typedef unsigned u32x4 __attribute__((ext_vector_type(4)));
typedef unsigned u32x2 __attribute__((ext_vector_type(2)));
typedef __bf16 bf16x2_t __attribute__((ext_vector_type(2)));
#define GAS __attribute__((address_space(1)))
#define LAS __attribute__((address_space(3)))
typedef GAS unsigned gu32;
typedef volatile LAS unsigned* lflag;
#define RLX_AGENT __ATOMIC_RELAXED, __HIP_MEMORY_SCOPE_AGENT

constexpr int D_ = 448, DH = 28, F_ = 1792;
constexpr int NT = 512;
constexpr unsigned KB = 1024;

constexpr unsigned CNT_X2 = 0;
constexpr unsigned CNT_O  = CNT_X2 + 32 * 128;
constexpr unsigned CNT_X1 = CNT_O + 256 * 128;
constexpr unsigned CNT_H  = CNT_X1 + 128 * 128;
constexpr unsigned CNT_KV = CNT_H + 128 * 128;
constexpr unsigned NCTRL  = CNT_KV / 128 + 512;
static_assert(NCTRL <= 1280, "k_prep clears five lines per workgroup");
constexpr unsigned OFF_WQKV = 160 * KB;
constexpr unsigned OFF_WO   = OFF_WQKV + 16 * 6 * 14 * KB;
constexpr unsigned OFF_WGU  = OFF_WO + 28 * 16 * KB;
constexpr unsigned OFF_WD   = OFF_WGU + 112 * 28 * KB;
constexpr unsigned OFF_CS   = OFF_WD + 7 * 56 * 4 * KB;
constexpr unsigned X_BYTES  = 128 * 28 * 512;
constexpr unsigned OFF_X    = OFF_CS + 32 * KB;
constexpr unsigned ST_BYTES = 128 * 28 * 128;
constexpr unsigned OFF_ST   = OFF_X + 5 * X_BYTES;
constexpr unsigned KV_BYTES = 16 * 64 * 4 * KB;
constexpr unsigned OFF_KV   = OFF_ST + 4 * ST_BYTES;
constexpr unsigned O_BYTES  = 128 * 16 * KB;
constexpr unsigned OFF_O    = OFF_KV + 2 * KV_BYTES;
constexpr unsigned H_BYTES  = 128 * 56 * KB;
constexpr unsigned OFF_H    = OFF_O + 2 * O_BYTES;
constexpr unsigned WS_USED  = OFF_H + 2 * H_BYTES;
constexpr unsigned OFF_STAMP = 64u << 20;

constexpr int L_REG = 80 * 1024;
constexpr int L_HALO = 24 * 1024, L_OWN = 56 * 1024, L_Q = 72 * 1024;
constexpr int L_RES = 136 * 1024;
constexpr int L_ST = L_RES + 8192;
constexpr int L_CS = L_ST + 1024;
constexpr int L_FL = L_CS + 2048;
constexpr int L_STAMP = L_FL + 256;
constexpr int L_BYTES = L_STAMP + 8 * 128 * 4;
enum { F_RDY = 0, F_SRDY = 2, F_ARR1 = 4, F_ARR3 = 5, F_ARR4 = 6, F_ARR5 = 8, F_RED = 10, F_ABORT = 12, F_GO2 = 13, F_GO3 = 14, F_GO4 = 15, F_P1A = 16, F_P1B = 17, F_P1C = 18, F_X2 = 19, F_KVD = 20 };
constexpr int p1_off(int r, int p) { return (r < 2 ? 24 + 16 * r : r == 2 ? 0 : r < 6 ? 80 + 16 * (r - 3) : p < 2 ? 16 : 120) * 1024 + p * 4096; }

__device__ __forceinline__ f32x4 mfma(u32x4 a, u32x4 b, f32x4 c) {
  return __builtin_amdgcn_mfma_f32_16x16x32_bf16(__builtin_bit_cast(bf16x8, a), __builtin_bit_cast(bf16x8, b), c, 0, 0, 0);
}
__device__ __forceinline__ f32x16 mfma32(u32x4 a, u32x4 b, f32x16 c) {
  return __builtin_amdgcn_mfma_f32_32x32x16_bf16(__builtin_bit_cast(bf16x8, a), __builtin_bit_cast(bf16x8, b), c, 0, 0, 0);
}
__device__ __forceinline__ unsigned pk(float a, float b) {
  bf16x2_t v = {(__bf16)a, (__bf16)b};
  return __builtin_bit_cast(unsigned, v);
}
__device__ __forceinline__ unsigned pks(float a, float b) {
  unsigned r;
  asm("v_cvt_pk_bf16_f32 %0, %1, %2" : "=v"(r) : "v"(a), "v"(b));
  return r;
}
__device__ __forceinline__ float bflo(unsigned u) { return __builtin_bit_cast(float, u << 16); }
__device__ __forceinline__ float bfhi(unsigned u) { return __builtin_bit_cast(float, u & 0xffff0000u); }
__device__ __forceinline__ unsigned f2u(float f) { return __builtin_bit_cast(unsigned, f); }
__device__ __forceinline__ float u2f(unsigned u) { return __builtin_bit_cast(float, u); }
__device__ __forceinline__ u32x4 lds16(const char* p) { return *(const u32x4*)p; }
#define FW(a, b) (a)
#define MAP1 4
#define MAP3 8
#define POLL_SLEEP "0"
#define YS 17
#define QGO 20
#define SCHED() __builtin_amdgcn_sched_barrier(0)
#define OPQ(x) asm("" : "+v"(x))
#define PRIO(n) __builtin_amdgcn_s_setprio(n)
#define WAIT_VM(n) asm volatile("s_waitcnt vmcnt(" #n ")" ::: "memory")
#define WAIT_ALL() asm volatile("s_waitcnt vmcnt(0) lgkmcnt(0)" ::: "memory")
#define WAIT_LGKM() asm volatile("s_waitcnt lgkmcnt(0)" ::: "memory")
typedef __amdgpu_buffer_rsrc_t rsrc_t;
__device__ __forceinline__ u32x4 ldw(rsrc_t rs, unsigned voff, unsigned soff) { return __builtin_amdgcn_raw_buffer_load_b128(rs, voff, soff, 0); }
__device__ __forceinline__ u32x4 ldh(rsrc_t rs, unsigned voff, unsigned soff) { return __builtin_amdgcn_raw_buffer_load_b128(rs, voff, soff, 16); }
__device__ __forceinline__ u32x4 ldwf(rsrc_t rs, unsigned l16, unsigned base, int k) { return ldw(rs, l16 + (k & 3) * 1024, base + (k >> 2) * 4096); }
#define WCNT(n) #n
#define WAIT_W(n) asm volatile("s_waitcnt vmcnt(" WCNT(n) ") lgkmcnt(0)" ::: "memory")
__device__ __forceinline__ u32x4 ldhf(rsrc_t rs, unsigned l16, unsigned base, int k) { return ldh(rs, l16 + (k & 3) * 1024, base + (k >> 2) * 4096); }
__device__ __forceinline__ unsigned opaque(unsigned v) { asm volatile("" : "+s"(v)); return v; }
template <int N> __device__ __forceinline__ void touch(const u32x4* w) {
#pragma unroll
  for (int k = 0; k < N; ++k) asm volatile("" :: "v"(w[k]));
}
template <int N> __device__ __forceinline__ void undef(u32x4* w) {
#pragma unroll
  for (int k = 0; k < N; ++k) asm volatile("" : "=v"(w[k]));
}
__device__ __forceinline__ int opaque_v(int v) { asm volatile("" : "+v"(v)); return v; }
__device__ __forceinline__ void sth(u32x4 v, char* ws, unsigned voff, unsigned soff) {
  asm volatile("global_store_dwordx4 %0, %1, %2 sc1\n\ts_nop 1" :: "v"(voff + soff), "v"(v), "s"(ws) : "memory");
}
__device__ __forceinline__ void sth32(unsigned v, char* ws, unsigned voff, unsigned soff) {
  asm volatile("global_store_dword %0, %1, %2 sc1" :: "v"(voff + soff), "v"(v), "s"(ws) : "memory");
}

#define STAMP() do {} while (0)

typedef short s16x4 __attribute__((ext_vector_type(4)));
__device__ __forceinline__ u32x2 ldtr(const char* p) {
  return __builtin_bit_cast(u32x2, __builtin_amdgcn_ds_read_tr16_b64_v4i16((LAS s16x4*)(LAS char*)p));
}
__device__ __forceinline__ float rbf(float v) { return bflo(pk(v, v) & 0xffffu); }
__device__ __forceinline__ float sum8(u32x4 v) {
  return ((bflo(v.x) + bfhi(v.x)) + (bflo(v.y) + bfhi(v.y))) + ((bflo(v.z) + bfhi(v.z)) + (bflo(v.w) + bfhi(v.w)));
}
__device__ __forceinline__ float dpp_add(float v, const int ctrl) {
  switch (ctrl) {
    case 0: return v + u2f(__builtin_amdgcn_update_dpp(0u, f2u(v), 0xB1, 0xf, 0xf, false));
    case 1: return v + u2f(__builtin_amdgcn_update_dpp(0u, f2u(v), 0x4E, 0xf, 0xf, false));
    case 2: return v + u2f(__builtin_amdgcn_update_dpp(0u, f2u(v), 0x124, 0xf, 0xf, false));
    default: return v + u2f(__builtin_amdgcn_update_dpp(0u, f2u(v), 0x128, 0xf, 0xf, false));
  }
}
__device__ __forceinline__ float sum32(float v) { auto r = __builtin_amdgcn_permlane32_swap(f2u(v), f2u(v), false, false); return u2f(r[0]) + u2f(r[1]); }
__device__ __forceinline__ float sum16(float v) { auto r = __builtin_amdgcn_permlane16_swap(f2u(v), f2u(v), false, false); return u2f(r[0]) + u2f(r[1]); }
__device__ __forceinline__ float wave_sum(float v) { return sum32(sum16(dpp_add(dpp_add(dpp_add(dpp_add(v, 0), 1), 2), 3))); }

#define PST16(v, off) sth((v), ws, (off), 0u)
constexpr int MAIN_LINES = 256;
constexpr int PW_STRIDE = 80, PW_BYTES = 452 * PW_STRIDE, PX_OFF = PW_BYTES, PR_OFF = PX_OFF + 2 * 7168;
__global__ __launch_bounds__(NT) void k_prep(const uint16_t* __restrict__ x, const uint16_t* __restrict__ wqkv,
                                             const uint16_t* __restrict__ wo, const uint16_t* __restrict__ wg,
                                             const uint16_t* __restrict__ wu, const uint16_t* __restrict__ wd, char* ws) {
  __shared__ __attribute__((aligned(16))) char sm[PR_OFF + 8 * 32 * 4];
  const int b = blockIdx.x, tid = threadIdx.x, lane = tid & 63, wave = __builtin_amdgcn_readfirstlane(tid >> 6);
  if (tid < 5) *(unsigned*)(ws + (b * 5 + tid) * 128) = 0u;
  unsigned code_word;
  {
    const char* pc; int delta;
    asm volatile("s_getpc_b64 %0\n1:\n\ts_mov_b32 %1, _Z6k_mainPcPt-1b" : "=s"(pc), "=s"(delta));
    const char* code = pc + delta;
    code_word = *(const GAS unsigned*)(code + min(tid, MAIN_LINES - 1) * 128);
  }
  const u32x4 xv = *(const u32x4*)(x + (size_t)(b * 8 + wave) * D_ + min(lane, 55) * 8);
  const uint16_t* src = wo; int ld = D_, ncols = 32, type = 3; char* dst = ws; int csf = -1, csb = 0, s_stride = 0, nt_stride = 0;
  if (b < 48) {
    int h = b / 3, p = b % 3; type = 0;
    src = wqkv + p * D_ + DH * h; ld = 3 * D_; ncols = DH;
    dst = ws + OFF_WQKV + (size_t)(h * 3 + p) * 28 * KB; s_stride = 1024; nt_stride = 14 * 1024;
    csf = h * 112 + p * 32;
  } else if (b < 62) {
    int j = b - 48; type = 1;
    src = wo + 32 * j;
    dst = ws + OFF_WO + (size_t)(2 * j) * 16 * KB; s_stride = 1024; nt_stride = 16 * 1024;
  } else if (b < 174) {
    int j = (b - 62) >> 1, u = (b - 62) & 1; type = 2;
    src = (u ? wu : wg) + 32 * j; ld = F_;
    dst = ws + OFF_WGU + (size_t)(2 * j) * 28 * KB + u * 256; s_stride = 1024; nt_stride = 28 * 1024;
    csf = 32 * j; csb = 96 + 112 * u;
  } else if (b < 230) {
    int j = (b - 174) >> 2, q = (b - 174) & 3; type = 2;
    src = wd + (size_t)q * 448 * D_ + 32 * j;
    dst = ws + OFF_WD + ((size_t)(j >> 1) * 112 + q * 28) * 2 * KB + (j & 1) * KB; s_stride = 2048; nt_stride = 256;
  }
  u32x2 wv[7];
  const int c4 = tid & 7;
  const bool cok = c4 * 4 < ncols;
#pragma unroll
  for (int u = 0; u < 7; ++u) wv[u] = *(const u32x2*)(src + (size_t)(u * 64 + (tid >> 3)) * ld + (cok ? c4 * 4 : 0));
  {
    const float e[8] = {bflo(xv.x), bfhi(xv.x), bflo(xv.y), bfhi(xv.y), bflo(xv.z), bfhi(xv.z), bflo(xv.w), bfhi(xv.w)};
    const float mean = wave_sum(lane < 56 ? sum8(xv) : 0.f) * (1.f / D_);
    float q = 0.f;
#pragma unroll
    for (int k = 0; k < 8; ++k) q += (e[k] - mean) * (e[k] - mean);
    const float var = wave_sum(lane < 56 ? q : 0.f) * (1.f / D_);
    const float mb = rbf(mean), sd = rbf(__builtin_sqrtf(rbf(rbf(var) + 1e-5f)));
    const float isd = 1.f / sd;
    float y[8];
#pragma unroll
    for (int k = 0; k < 8; ++k) y[k] = rbf(e[k] - mb) * isd;
    if (lane < 56) {
      char* d = sm + PX_OFF + (lane >> 1) * 256 + wave * 16 + (lane & 1) * 8;
      *(u32x2*)d = u32x2{xv.x, xv.y}; *(u32x2*)(d + 128) = u32x2{xv.z, xv.w};
      *(u32x2*)(d + 7168) = u32x2{pk(y[0], y[1]), pk(y[2], y[3])}; *(u32x2*)(d + 7168 + 128) = u32x2{pk(y[4], y[5]), pk(y[6], y[7])};
    }
  }
#pragma unroll
  for (int u = 0; u < 7; ++u) *(u32x2*)(sm + (u * 64 + (tid >> 3)) * PW_STRIDE + c4 * 8) = cok ? wv[u] : u32x2{0u, 0u};
  if (tid < 40) *(u32x2*)(sm + 448 * PW_STRIDE + tid * 8) = u32x2{0u, 0u};
  asm volatile("" :: "v"(code_word));
  __syncthreads();
  if (tid < 448) {
    const unsigned o = ((b >> 2) * 28 + (tid >> 4)) * KB + ((b >> 1) & 1) * 512 + ((tid >> 3) & 1) * 256 + (b & 1) * 128 + (tid & 7) * 16;
    PST16(lds16(sm + PX_OFF + tid * 16), OFF_X + o);
    PST16(lds16(sm + PX_OFF + 7168 + tid * 16), OFF_X + 4 * X_BYTES + o);
  }
  if (type == 3) return;
  const int i = lane & 15, g = lane >> 4, q4 = i >> 2, p4 = i & 3;
  float csum = 0.f;
#pragma unroll
  for (int k = 0; k < 4; ++k) {
    const int f = wave + 8 * k;
    if (f < (type == 1 ? 32 : 28)) {
      int s, nt, r0, r1;
      if (type == 2) { s = f; nt = g & 1; r0 = 16 * s + 4 * (g >> 1); r1 = r0 + 8; }
      else if (type == 0) { s = f >> 1; nt = f & 1; r0 = 32 * s + 16 * (g >> 1) + 4 * (g & 1); r1 = r0 + 8; }
      else { s = f >> 1; nt = f & 1; r0 = DH * s + 4 * g; r1 = g < 3 ? r0 + 16 : 448; }
      const u32x2 lo = ldtr(sm + (r0 + q4) * PW_STRIDE + nt * 32 + p4 * 8), hi = ldtr(sm + (r1 + q4) * PW_STRIDE + nt * 32 + p4 * 8);
      const u32x4 o = {lo.x, lo.y, hi.x, hi.y};
      PST16(o, (unsigned)(dst - ws) + s * s_stride + nt * nt_stride + (type == 2 ? i * 16 + (g >> 1) * 512 : lane * 16));
      csum += sum8(o);
    }
  }
  if (csf >= 0) {
    csum = sum32(csum);
    if (type == 0) csum = sum16(csum);
    float* red = (float*)(sm + PR_OFF);
    if (lane < 32) red[wave * 32 + lane] = (type == 0 && (lane >> 4) != (wave & 1)) ? 0.f : csum;
    __syncthreads();
    if (tid < 32) {
      float t = 0.f;
#pragma unroll
      for (int w = 0; w < 8; ++w) t += red[w * 32 + tid];
      const int f = csf + tid;
      ((float*)(ws + OFF_CS))[(f / 112) * 512 + csb + f % 112] = t;
    }
  }
}

__device__ __forceinline__ void lwait(lflag fl, int idx, unsigned tgt) {
  unsigned v, sv, n = 1u << 16;
  const unsigned addr = (unsigned)(uintptr_t)(fl + idx);
  asm volatile("1:\n\tds_read_b32 %0, %3\n\ts_waitcnt lgkmcnt(0)\n\tv_readfirstlane_b32 %1, %0\n\ts_cmp_ge_u32 %1, %4\n\t"
               "s_cbranch_scc1 2f\n\ts_sleep 1\n\ts_sub_u32 %2, %2, 1\n\ts_cbranch_scc0 1b\n2:"
               : "=&v"(v), "=&s"(sv), "+s"(n) : "v"(addr), "s"(tgt) : "memory", "scc");
}
__device__ __forceinline__ bool lready(lflag fl, int idx, unsigned tgt) {
  bool r = (unsigned)__builtin_amdgcn_readfirstlane(fl[idx]) >= tgt;
  asm volatile("" ::: "memory");
  return r;
}
__device__ __forceinline__ bool arrive(lflag fl, int idx, unsigned tgt) {
  unsigned old, so;
  const unsigned addr = (unsigned)(uintptr_t)(fl + idx), one = 1u;
  asm volatile("s_mov_b64 exec, 1\n\tds_add_rtn_u32 %0, %2, %3\n\ts_mov_b64 exec, -1\n\ts_waitcnt lgkmcnt(0)\n\tv_readfirstlane_b32 %1, %0"
               : "=&v"(old), "=s"(so) : "v"(addr), "v"(one) : "memory");
  return so + 1u == tgt;
}
__device__ __forceinline__ void arrive_nr(lflag fl, int idx) {
  const unsigned addr = (unsigned)(uintptr_t)(fl + idx), one = 1u;
  asm volatile("s_mov_b64 exec, 1\n\tds_add_u32 %0, %1\n\ts_mov_b64 exec, -1" :: "v"(addr), "v"(one) : "memory");
}
__device__ __forceinline__ void gadd(char* ws, unsigned off) {
  const unsigned one = 1u;
  asm volatile("s_mov_b64 exec, 1\n\tglobal_atomic_add %0, %1, %2\n\ts_mov_b64 exec, -1" :: "v"(off), "v"(one), "s"(ws) : "memory");
}
__device__ __forceinline__ void gpoll(char* ws, unsigned off0, unsigned off1, unsigned tgt) {
  unsigned a, c, n = 1u << 15;
  off0 = __builtin_amdgcn_readfirstlane(off0); off1 = __builtin_amdgcn_readfirstlane(off1);
  asm volatile("s_nop 4\n1:\n\ts_load_dword %0, %3, %4 glc\n\ts_load_dword %1, %3, %5 glc\n\ts_waitcnt lgkmcnt(0)\n\ts_min_u32 %0, %0, %1\n\t"
               "s_cmp_ge_u32 %0, %6\n\ts_cbranch_scc1 2f\n\ts_sleep " POLL_SLEEP "\n\ts_sub_u32 %2, %2, 1\n\ts_cbranch_scc0 1b\n2:"
               : "=&s"(a), "=&s"(c), "+s"(n) : "s"(ws), "s"(off0), "s"(off1), "s"(tgt) : "memory", "scc");
}
__device__ __forceinline__ bool gready(char* ws, unsigned off, unsigned tgt) {
  unsigned a;
  off = __builtin_amdgcn_readfirstlane(off);
  asm volatile("s_nop 4\n\ts_load_dword %0, %1, %2 glc\n\ts_waitcnt lgkmcnt(0)" : "=&s"(a) : "s"(ws), "s"(off) : "memory");
  return a >= tgt;
}
__device__ __forceinline__ void warm(const char* ws, unsigned off, int nlines, int lane) {
  unsigned v, a[7];
#pragma unroll
  for (int k = 0; k < 7; ++k) a[k] = off + min(k * 64 + lane, nlines - 1) * 128;
  asm volatile("global_load_dword %0, %1, %8 sc1\n\tglobal_load_dword %0, %2, %8 sc1\n\tglobal_load_dword %0, %3, %8 sc1\n\t"
               "global_load_dword %0, %4, %8 sc1\n\tglobal_load_dword %0, %5, %8 sc1\n\tglobal_load_dword %0, %6, %8 sc1\n\t"
               "global_load_dword %0, %7, %8 sc1\n\ts_waitcnt vmcnt(0)"
               : "=&v"(v) : "v"(a[0]), "v"(a[1]), "v"(a[2]), "v"(a[3]), "v"(a[4]), "v"(a[5]), "v"(a[6]), "s"(ws) : "memory");
}
#define BAR() do { asm volatile("s_waitcnt lgkmcnt(0)" ::: "memory"); __builtin_amdgcn_s_barrier(); asm volatile("" ::: "memory"); } while (0)
__device__ __forceinline__ void dma8k(const char* ws, unsigned voff, unsigned lds) {
  unsigned keep;
  lds = __builtin_amdgcn_readfirstlane(lds + 4096);
  asm volatile("s_mov_b32 %0, m0\n\ts_mov_b32 m0, %3\n\ts_nop 0\n\t"
               "global_load_lds_dwordx4 %1, %2 offset:-4096 sc1\n\tglobal_load_lds_dwordx4 %1, %2 offset:-3072 sc1\n\t"
               "global_load_lds_dwordx4 %1, %2 offset:-2048 sc1\n\tglobal_load_lds_dwordx4 %1, %2 offset:-1024 sc1\n\t"
               "global_load_lds_dwordx4 %1, %2 sc1\n\tglobal_load_lds_dwordx4 %1, %2 offset:1024 sc1\n\t"
               "global_load_lds_dwordx4 %1, %2 offset:2048 sc1\n\tglobal_load_lds_dwordx4 %1, %2 offset:3072 sc1\n\t"
               "s_mov_b32 m0, %0"
               : "=&s"(keep) : "v"(voff + 4096), "s"(ws), "s"(lds) : "memory");
}
__device__ __forceinline__ void dma4k(const char* ws, unsigned voff, unsigned lds) {
  unsigned keep;
  lds = __builtin_amdgcn_readfirstlane(lds);
  asm volatile("s_mov_b32 %0, m0\n\ts_mov_b32 m0, %3\n\ts_nop 0\n\t"
               "global_load_lds_dwordx4 %1, %2 sc1\n\tglobal_load_lds_dwordx4 %1, %2 offset:1024 sc1\n\t"
               "global_load_lds_dwordx4 %1, %2 offset:2048 sc1\n\tglobal_load_lds_dwordx4 %1, %2 offset:3072 sc1\n\t"
               "s_mov_b32 m0, %0"
               : "=&s"(keep) : "v"(voff), "s"(ws), "s"(lds) : "memory");
}
__device__ __forceinline__ void dma1k(const char* ws, unsigned voff, unsigned lds) {
  unsigned keep;
  lds = __builtin_amdgcn_readfirstlane(lds);
  asm volatile("s_mov_b32 %0, m0\n\ts_mov_b32 m0, %3\n\ts_nop 0\n\tglobal_load_lds_dwordx4 %1, %2 sc1\n\ts_mov_b32 m0, %0"
               : "=&s"(keep) : "v"(voff), "s"(ws), "s"(lds) : "memory");
}

constexpr float GC1 = -2.f * 0.7978845608028654f * 1.4426950408889634f, GC2 = GC1 * 0.044715f;
__device__ __forceinline__ u32x4 pair16(u32x2 a, u32x2 b) {
  auto rx = __builtin_amdgcn_permlane32_swap(a.x, b.x, false, false);
  auto ry = __builtin_amdgcn_permlane32_swap(a.y, b.y, false, false);
  return u32x4{rx[0], ry[0], rx[1], ry[1]};
}
__device__ __forceinline__ float allg_max(float m) {
  auto r = __builtin_amdgcn_permlane32_swap(f2u(m), f2u(m), false, false);
  m = fmaxf(u2f(r[0]), u2f(r[1]));
  auto q = __builtin_amdgcn_permlane16_swap(f2u(m), f2u(m), false, false);
  return fmaxf(u2f(q[0]), u2f(q[1]));
}

__device__ __forceinline__ void dma_x(char* ws, unsigned smb, int xi, int T0, int X, int lane) {
  const unsigned src = OFF_X + xi * X_BYTES + (unsigned)T0 * 14 * KB + lane * 16;
#pragma unroll
  for (int k = 0; k < 7; ++k) dma8k(ws, src + k * 8192, smb + X * L_REG + k * 8192);
}
__device__ __forceinline__ void stat_loads(char* ws, u32x4 (&p)[14], int xi, int T0, int lane) {
  const unsigned vo = (lane >> 4) * 3584 + (lane & 15) * 16 + OFF_ST + xi * ST_BYTES + (unsigned)T0 * 3584;
  asm volatile("global_load_dwordx4 %0, %14, %15 sc1\n\tglobal_load_dwordx4 %1, %14, %15 offset:256 sc1\n\t"
               "global_load_dwordx4 %2, %14, %15 offset:512 sc1\n\tglobal_load_dwordx4 %3, %14, %15 offset:768 sc1\n\t"
               "global_load_dwordx4 %4, %14, %15 offset:1024 sc1\n\tglobal_load_dwordx4 %5, %14, %15 offset:1280 sc1\n\t"
               "global_load_dwordx4 %6, %14, %15 offset:1536 sc1\n\tglobal_load_dwordx4 %7, %14, %15 offset:1792 sc1\n\t"
               "global_load_dwordx4 %8, %14, %15 offset:2048 sc1\n\tglobal_load_dwordx4 %9, %14, %15 offset:2304 sc1\n\t"
               "global_load_dwordx4 %10, %14, %15 offset:2560 sc1\n\tglobal_load_dwordx4 %11, %14, %15 offset:2816 sc1\n\t"
               "global_load_dwordx4 %12, %14, %15 offset:3072 sc1\n\tglobal_load_dwordx4 %13, %14, %15 offset:3328 sc1"
               : "=&v"(p[0]), "=&v"(p[1]), "=&v"(p[2]), "=&v"(p[3]), "=&v"(p[4]), "=&v"(p[5]), "=&v"(p[6]), "=&v"(p[7]), "=&v"(p[8]),
                 "=&v"(p[9]), "=&v"(p[10]), "=&v"(p[11]), "=&v"(p[12]), "=&v"(p[13])
               : "v"(vo), "s"(ws) : "memory");
}
__device__ __forceinline__ bool stat_wait_poll(char* ws, u32x4 (&p)[14], unsigned off, unsigned tgt) {
  unsigned v;
  asm volatile("global_load_dword %0, %15, %16 sc1\n\ts_waitcnt vmcnt(0)"
               : "=&v"(v), "+v"(p[0]), "+v"(p[1]), "+v"(p[2]), "+v"(p[3]), "+v"(p[4]), "+v"(p[5]), "+v"(p[6]), "+v"(p[7]), "+v"(p[8]), "+v"(p[9]),
                 "+v"(p[10]), "+v"(p[11]), "+v"(p[12]), "+v"(p[13]) : "v"(off), "s"(ws) : "memory");
  return __all(v >= tgt);
}
__device__ __forceinline__ void stat_landed(u32x4 (&p)[14]) {
  asm volatile("" : "+v"(p[0]), "+v"(p[1]), "+v"(p[2]), "+v"(p[3]), "+v"(p[4]), "+v"(p[5]), "+v"(p[6]), "+v"(p[7]), "+v"(p[8]), "+v"(p[9]),
                    "+v"(p[10]), "+v"(p[11]), "+v"(p[12]), "+v"(p[13]) :: "memory");
}
template <int P0> __device__ __forceinline__ void dma_rounds(char* ws, unsigned smb, unsigned src, int r0, int r1) {
#pragma unroll
  for (int r = r0; r < r1; ++r)
#pragma unroll
    for (int p = P0; p < P0 + 2; ++p) dma4k(ws, src + p * 28 * KB + r * 4096, smb + p1_off(r, p));
}
__device__ __forceinline__ void stat_finish(char* sm, lflag fl, const u32x4 (&p)[14], int X, unsigned stage, int lane) {
  float a[4];
#pragma unroll
  for (int r = 0; r < 4; ++r) {
    float t0 = u2f(p[0][r]) + u2f(p[1][r]), t1 = u2f(p[2][r]) + u2f(p[3][r]);
    OPQ(t0); OPQ(t1);
#pragma unroll
    for (int k = 4; k < 14; k += 2) { t0 += u2f(p[k][r]); t1 += u2f(p[k + 1][r]); OPQ(t0); OPQ(t1); }
    a[r] = t0 + t1;
  }
  float ms[4], rsd[4];
#pragma unroll
  for (int r = 0; r < 4; ++r) {
    float s1 = a[r] + u2f(__builtin_amdgcn_update_dpp(0u, f2u(a[r]), 0x128, 0xf, 0xf, false));
    float s2 = u2f(__builtin_amdgcn_update_dpp(0u, f2u(s1), 0x124, 0xf, 0xf, false));
    float mean = s1 * (1.f / D_);
    OPQ(mean);
    float var = fmaxf(__builtin_fmaf(-mean, mean, s2 * (1.f / D_)), 0.f);
    ms[r] = mean; rsd[r] = __builtin_amdgcn_rsqf(var + 1e-5f);
  }
  if ((lane & 15) < 4) {
    char* d = sm + L_ST + X * 512 + ((lane >> 4) * 16 + (lane & 3) * 4) * 8;
    *(f32x4*)d = f32x4{ms[0], rsd[0], ms[1], rsd[1]};
    *(f32x4*)(d + 16) = f32x4{ms[2], rsd[2], ms[3], rsd[3]};
  }
  asm volatile("" ::: "memory");
  fl[F_SRDY + X] = stage;
}

__device__ __forceinline__ void serve_x(char* ws, char* sm, unsigned smb, lflag fl, int xi, int c, unsigned cnt, unsigned tgt, int lfA, int lfB,
                                        unsigned ltgt, unsigned stage, int lane) {
  u32x4 p[14];
  lwait(fl, lfA, ltgt);
  gpoll(ws, cnt, cnt + 128, tgt);
  dma_x(ws, smb, xi, 8 * c, 0, lane);
  stat_loads(ws, p, xi, 8 * c, lane);
  WAIT_VM(14);
  fl[F_RDY] = stage;
  const bool now = stat_wait_poll(ws, p, cnt + (2 + (lane & 1)) * 128, tgt) && lready(fl, lfB, ltgt);
  if (now) dma_x(ws, smb, xi, 8 * c + 4, 1, lane);
  stat_finish(sm, fl, p, 0, stage, lane);
  if (!now) {
    lwait(fl, lfB, ltgt);
    gpoll(ws, cnt + 256, cnt + 384, tgt);
    dma_x(ws, smb, xi, 8 * c + 4, 1, lane);
  }
  stat_loads(ws, p, xi, 8 * c + 4, lane);
  WAIT_VM(14);
  fl[F_RDY + 1] = stage;
  (void)stat_wait_poll(ws, p, cnt, 0u);
  stat_finish(sm, fl, p, 1, stage, lane);
}

template <class PRE>
__device__ __forceinline__ u32x4 attn_tile(const char* kv, u32x4 q, int qt, int lane, PRE pre) {
  const int i = lane & 15, g = lane >> 4;
  const float NEG = -__builtin_inff();
  f32x4 s[18];
  u32x4 kf[18];
#pragma unroll
  for (int j = 0; j < 6; ++j) kf[j] = lds16(kv + (j >> 1) * 4096 + (j & 1) * 1024);
#pragma unroll
  for (int j = 0; j < 18; ++j) {
    if (j + 6 < 18) kf[j + 6] = lds16(kv + ((j + 6) >> 1) * 4096 + (j & 1) * 1024);
    SCHED();
    s[j] = mfma(kf[j], q, f32x4{0.f, 0.f, 0.f, 0.f});
    if (j % 2 == 1) pre(j / 2);
    SCHED();
  }
  u32x4 vf[18];
#pragma unroll
  for (int j = 0; j < 4; ++j) vf[j] = lds16(kv + (j >> 1) * 4096 + 2048 + (j & 1) * 1024);
  const int dq = 16 * (qt & 1) + i - 4 * g;
#pragma unroll
  for (int u = 0; u < 2; ++u)
#pragma unroll
    for (int r = 0; r < 4; ++r) {
      const bool lo = dq < 16 * u + r;
      s[u][r] = lo ? s[u][r] : NEG;
      s[16 + u][r] = lo ? NEG : s[16 + u][r];
    }
  float mx[4];
#pragma unroll
  for (int r = 0; r < 4; ++r) mx[r] = fmaxf(s[2][r], s[3][r]);
#pragma unroll
  for (int j = 4; j < 20; j += 2)
#pragma unroll
    for (int r = 0; r < 4; ++r) mx[r] = fmaxf(fmaxf(mx[r], s[j % 18][r]), s[(j + 1) % 18][r]);
  const float m = allg_max(fmaxf(fmaxf(mx[0], mx[1]), fmaxf(mx[2], mx[3])));
  constexpr float SC = 0.18898223650461361f * 1.4426950408889634f;
  const float mc = -m * SC;
  const f32x4 m4 = {mc, mc, mc, mc}, c4 = {SC, SC, SC, SC};
  SCHED();
#pragma unroll
  for (int j = 0; j < 18; ++j) s[j] = __builtin_elementwise_fma(s[j], c4, m4);
  pre(9);
  SCHED();
  f32x4 o0 = {0.f, 0.f, 0.f, 0.f}, o1 = {0.f, 0.f, 0.f, 0.f};
#pragma unroll
  for (int j = 0; j < 9; ++j) {
    if (j + 2 < 9) { vf[2 * j + 4] = lds16(kv + (j + 2) * 4096 + 2048); vf[2 * j + 5] = lds16(kv + (j + 2) * 4096 + 3072); }
    float p[8];
#pragma unroll
    for (int k = 0; k < 8; ++k) p[k] = __builtin_amdgcn_exp2f(s[2 * j + (k >> 2)][k & 3]);
    const u32x4 pf = {pk(p[0], p[1]), pk(p[2], p[3]), pk(p[4], p[5]), pk(p[6], p[7])};
    o0 = mfma(vf[2 * j], pf, o0);
    o1 = mfma(vf[2 * j + 1], pf, o1);
    if (j < 6) { pre(10 + 2 * j); pre(11 + 2 * j); }
    SCHED();
  }
  auto r = __builtin_amdgcn_permlane32_swap(f2u(o1[0]), f2u(o1[0]), false, false);
  auto t = __builtin_amdgcn_permlane16_swap(r[1], r[1], false, false);
  const float inv = __builtin_amdgcn_rcpf(u2f(t[1]));
  return u32x4{pk(o0[0] * inv, o0[1] * inv), pk(o0[2] * inv, o0[3] * inv), pk(o1[0] * inv, o1[1] * inv), pk(o1[2] * inv, o1[3] * inv)};
}

__device__ __forceinline__ u32x4 geglu8(const f32x16& acc, f32x2 ms, const f32x4& cg0, const f32x4& cg1, const f32x4& cu0, const f32x4& cu1) {
  const float q = ms.y * ms.y, k1 = GC1 * ms.y, k2 = GC2 * ms.y * q, ir = __builtin_amdgcn_rcpf(q), nm = -ms.x;
  float a[8], b[8], t[8];
#pragma unroll
  for (int r = 0; r < 8; ++r) { a[r] = __builtin_fmaf(nm, r < 4 ? cg0[r & 3] : cg1[r & 3], acc[r]); OPQ(a[r]); }
  SCHED();
#pragma unroll
  for (int r = 0; r < 8; ++r) { t[r] = a[r] * a[r]; OPQ(t[r]); }
  SCHED();
#pragma unroll
  for (int r = 0; r < 8; ++r) { t[r] = __builtin_fmaf(t[r], k2, k1); OPQ(t[r]); }
  SCHED();
#pragma unroll
  for (int r = 0; r < 8; ++r) { t[r] = t[r] * a[r]; OPQ(t[r]); }
  SCHED();
#pragma unroll
  for (int r = 0; r < 8; ++r) {
    t[r] = __builtin_amdgcn_exp2f(t[r]);
    b[r] = __builtin_fmaf(nm, r < 4 ? cu0[r & 3] : cu1[r & 3], acc[r + 8]); OPQ(b[r]);
  }
  SCHED();
#pragma unroll
  for (int r = 0; r < 8; ++r) { t[r] = __builtin_fmaf(t[r], ir, ir); OPQ(t[r]); }
  SCHED();
#pragma unroll
  for (int r = 0; r < 8; ++r) { t[r] = __builtin_amdgcn_rcpf(t[r]); b[r] = a[r] * b[r]; OPQ(b[r]); }
  SCHED();
#pragma unroll
  for (int r = 0; r < 8; ++r) { t[r] = t[r] * b[r]; OPQ(t[r]); }
  SCHED();
  return u32x4{pks(t[0], t[1]), pks(t[2], t[3]), pks(t[4], t[5]), pks(t[6], t[7])};
}

__device__ __forceinline__ void fin5(char* ws, uint16_t* out, char* sm, int X, int nt, int P, int J, int l, int lane) {
  const int i = lane & 15, g = lane >> 4;
  const u32x4 xr = lds16(sm + L_RES + X * 4096 + nt * 1024 + lane * 16);
  const char* red = sm + X * L_REG + nt * 2048 + lane * 16;
  f32x4 pa[7], pb[7];
#pragma unroll
  for (int k = 0; k < 7; ++k) { pa[k] = *(const f32x4*)(red + k * 8192); pb[k] = *(const f32x4*)(red + k * 8192 + 1024); }
  float v[8] = {bflo(xr.x), bfhi(xr.x), bflo(xr.y), bfhi(xr.y), bflo(xr.z), bfhi(xr.z), bflo(xr.w), bfhi(xr.w)};
  SCHED();
#pragma unroll
  for (int k = 0; k < 7; ++k) {
#pragma unroll
    for (int r = 0; r < 8; ++r) { v[r] += r < 4 ? pa[k][r] : pb[k][r - 4]; OPQ(v[r]); }
    SCHED();
  }
  const u32x4 o = {pks(v[0], v[1]), pks(v[2], v[3]), pks(v[4], v[5]), pks(v[6], v[7])};
  if (l == 0) {
    sth(o, ws, (g & 1) * 512 + (i + 16 * (g >> 1)) * 16, OFF_X + 2 * X_BYTES + (P * 28 + J) * KB);
    float s1 = v[0] + v[1], s2 = v[0] * v[0], s3 = v[2] + v[3], s4 = v[1] * v[1];
    OPQ(s1); OPQ(s2); OPQ(s3); OPQ(s4);
    s1 += v[4]; s3 += v[5]; s2 = __builtin_fmaf(v[2], v[2], s2); s4 = __builtin_fmaf(v[3], v[3], s4);
    OPQ(s1); OPQ(s2); OPQ(s3); OPQ(s4);
    s1 += v[6]; s3 += v[7]; s2 = __builtin_fmaf(v[4], v[4], s2); s4 = __builtin_fmaf(v[5], v[5], s4);
    OPQ(s1); OPQ(s2); OPQ(s3); OPQ(s4);
    s1 += s3; s2 = __builtin_fmaf(v[6], v[6], s2); s4 = __builtin_fmaf(v[7], v[7], s4);
    OPQ(s1); OPQ(s2); OPQ(s4);
    s2 += s4;
    auto r = __builtin_amdgcn_permlane32_swap(f2u(s1), f2u(s2), false, false);
    const float sv = u2f(r[0]) + u2f(r[1]);
    sth32(f2u(sv), ws, (g & 1) * 3584 + (lane >> 5) * 64 + i * 4, OFF_ST + 2 * ST_BYTES + (2 * P * 28 + J) * 128);
  } else {
    uint16_t* op = out + (size_t)(P * 32 + (lane & 31)) * D_ + 16 * J + 4 * (lane >> 5);
    *(u32x2*)op = u32x2{o.x, o.y};
    *(u32x2*)(op + 8) = u32x2{o.z, o.w};
  }
}

#define IDS()                                                                                                   \
  const int lane = opaque_v(lane0), wave = opaque(wave0), b = opaque(b0), xcd = b & 7, rk = b >> 3;             \
  const int c1 = MAP1 == 8 ? 2 * xcd + (rk & 1) : MAP1 == 4 ? 4 * (xcd >> 1) + (rk & 3) : 8 * (xcd >> 2) + (rk & 7); \
  const int h1 = MAP1 == 8 ? rk >> 1 : MAP1 == 4 ? 8 * (xcd & 1) + (rk >> 2) : 4 * (xcd & 3) + (rk >> 3);       \
  const int c3 = MAP3 == 8 ? 2 * xcd + (rk & 1) : 4 * (xcd >> 1) + (rk & 3);                                    \
  const int u5 = MAP3 == 8 ? rk >> 1 : 8 * (xcd & 1) + (rk >> 2);                                               \
  const int m3 = (u5 & 3) * 4 + (u5 >> 2);                                                                      \
  const bool act5 = u5 < 14;                                                                                    \
  const int th = u5 & 1, ng = u5 >> 1, i = lane & 15, g = lane >> 4;                                            \
  const int part = wave < 6 ? 2 - (wave >> 1) : 0, nt1 = wave & 1, nt6 = 2 * part + nt1, w7 = min(wave, 6), J4 = 7 * h1 + w7, J3 = 7 * (m3 & 3) + w7; \
  const unsigned l16 = lane * 16, half_off = (lane & 31) * 16, res_off = (i + 16 * (g & 1)) * 16 + 8 * (g >> 1), f32_off = (g & 1) * 512 + (i + 16 * (g >> 1)) * 16; \
  (void)c1; (void)h1; (void)c3; (void)act5; (void)ng; (void)th; (void)part; (void)nt1; (void)J4; (void)J3; (void)l16; (void)half_off; (void)res_off; (void)f32_off; (void)nt6
__global__ __launch_bounds__(NT, 2) void k_main(char* ws, uint16_t* out) {
  extern __shared__ __attribute__((aligned(16))) char sm[];
  const int tid = threadIdx.x, lane0 = tid & 63, wave0 = __builtin_amdgcn_readfirstlane(tid >> 6), b0 = blockIdx.x;
  IDS();
  lflag fl = (lflag)(LAS char*)(sm + L_FL);
  const rsrc_t rs = __builtin_amdgcn_make_buffer_rsrc(ws, 0, WS_USED, 0x00020000);
  int stn = 0; (void)stn;
  const unsigned smb = (unsigned)(uintptr_t)(LAS char*)sm;
  u32x4 w1[14];
  if (wave0 == 7) {
    PRIO(3);
    const unsigned src = OFF_X + 4 * X_BYTES + c1 * 112 * KB + l16;
    dma1k(ws, OFF_CS + h1 * 2048 + l16, smb + L_CS);
    dma1k(ws, OFF_CS + h1 * 2048 + 1024 + l16, smb + L_CS + 1024);
    dma_rounds<0>(ws, smb, src, 0, 7);
    fl[lane] = 0u;
    *(f32x2*)(sm + L_ST + lane * 8) = f32x2{0.f, 1.f};
    *(f32x2*)(sm + L_ST + 512 + lane * 8) = f32x2{0.f, 1.f};
    undef<14>(w1);
  } else if (wave0 < 6) {
#pragma unroll
    for (int s = 0; s < 14; ++s) w1[s] = ldwf(rs, l16, OFF_WQKV + (h1 * 6 + nt6) * 14 * KB, s);
  } else {
    dma_rounds<2>(ws, smb, OFF_X + 4 * X_BYTES + c1 * 112 * KB + l16, 0, 7);
    undef<14>(w1);
  }
  BAR();

#pragma unroll 1
  for (int l = 0; l < 2; ++l) {
    const unsigned st = 5 * l;
    u32x4 w3[16], w4[28], w5[32];
#define PRE1(k)                                                                                         \
  do {                                                                                                  \
    if ((k) < 16) w3[(k)] = ldwf(rs, l16, OFF_WO + J3 * 16 * KB, (k));                                  \
    else if ((k) < 44) w4[(k) - 16] = ldwf(rs, l16, OFF_WGU + J4 * 28 * KB, (k) - 16);                  \
    else w5[(k) - 44] = ldwf(rs, l16, OFF_WD + ((act5 ? ng : 0) * 112 + 16 * w7) * 2 * KB, (k) - 44);   \
  } while (0)
#define PRE34(k0, k1)                                                                                   \
  SCHED();                                                                                              \
  _Pragma("unroll") for (int k = k0; k < k1; ++k) PRE1(k);                                              \
  SCHED();
    if (wave0 == 7) {
      IDS();
      if (l == 0) {
        fl[F_SRDY] = 1u; fl[F_SRDY + 1] = 1u;
        WAIT_VM(40); arrive_nr(fl, F_P1A);
        WAIT_VM(24); arrive_nr(fl, F_P1B);
        WAIT_VM(0); arrive_nr(fl, F_P1C);
      } else {
        const unsigned src = OFF_X + 2 * X_BYTES + c1 * 112 * KB + l16;
        u32x4 pa[14], pb[14];
        if (act5) {
          lwait(fl, F_ARR5, 8);
          __builtin_amdgcn_s_sleep(6);
        } else {
          lwait(fl, F_GO4, 1);
#pragma unroll 1
          for (int k = 0; k < 64 && !gready(ws, CNT_X2 + c1 * 256, 1); ++k) __builtin_amdgcn_s_sleep(24);
        }
        gpoll(ws, CNT_X2 + c1 * 256, CNT_X2 + c1 * 256 + 128, 7);
        STAMP();
        fl[F_X2] = 1u;
        dma_rounds<0>(ws, smb, src, 0, 4);
        WAIT_VM(16); arrive_nr(fl, F_P1A);
        dma_rounds<0>(ws, smb, src, 4, 7);
        WAIT_VM(24); arrive_nr(fl, F_P1B);
        stat_loads(ws, pa, 2, 8 * c1, lane);
        stat_loads(ws, pb, 2, 8 * c1 + 4, lane);
        WAIT_VM(28); arrive_nr(fl, F_P1C);
        WAIT_VM(14);
        stat_landed(pa);
        stat_finish(sm, fl, pa, 0, st + 1, lane);
        WAIT_VM(0);
        stat_landed(pb);
        stat_finish(sm, fl, pb, 1, st + 1, lane);
        STAMP();
      }
      lwait(fl, F_ARR1, 6 * (l + 1));
#pragma unroll 1
      for (int k = 0; k < 8 - 4 * c1; ++k) {
        const u32x4 kz = {0u, 0u, g == 3 ? 0xff80u : 0u, 0u}, vz = {0u, 0u, 0u, 0u};
        char* d = sm + L_HALO + k * 4096 + l16;
        *(u32x4*)d = kz; *(u32x4*)(d + 1024) = kz; *(u32x4*)(d + 2048) = vz; *(u32x4*)(d + 3072) = vz;
      }
      if (c1 > 0) gpoll(ws, CNT_KV + ((l * 16 + h1) * 16 + max(c1 - 2, 0)) * 128, CNT_KV + ((l * 16 + h1) * 16 + c1 - 1) * 128, 4);
      STAMP();
      const unsigned src = OFF_KV + l * KV_BYTES + (h1 * 64 + 4 * c1 - 8) * 4096 + l16;
#pragma unroll
      for (int k = 0; k < 4; ++k) if (2 * c1 + k >= 4) dma8k(ws, src + k * 8192, smb + L_HALO + k * 8192);
      WAIT_VM(0);
      PRIO(0);
      undef<6>(w3);
    } else {
      if (wave0 < 6) touch<14>(w1);
      IDS();
      if (wave < 6) {
        if (wave < 4) lwait(fl, F_P1A, 2 * (l + 1)); else lwait(fl, F_KVD, 6 * l + 4);
        STAMP();
        const char* xs = sm + (g >> 1) * 1024 + half_off;
        const char* xs2 = sm + opaque_v(L_REG + (g >> 1) * 1024 + half_off);
        const char* stp = sm + L_ST;
        const unsigned so_kv = OFF_KV + l * KV_BYTES + (h1 * 64 + 4 * c1) * 4096;
        f32x4 acc[8];
        u32x4 xf[28][4];
#pragma unroll
        for (int t = 0; t < 8; ++t) acc[t] = f32x4{0.f, 0.f, 0.f, 0.f};
#define P1_O(u, t) (p1_off((u) >> 2, 2 * ((u) & 1) + ((t) >> 1)) + (((u) >> 1) & 1) * 2048 + ((t) & 1) * 512)
#define P1_X(u, t) lds16(P1_O(u, t) < L_REG ? xs + P1_O(u, t) : xs2 + (P1_O(u, t) - L_REG))
#define P1_LOOP(MM)                                                                                     \
  _Pragma("unroll") for (int u = 0; u < 2; ++u) {                                                       \
    _Pragma("unroll") for (int t = 0; t < 4; ++t) xf[u][t] = P1_X(u, t);                                \
  }                                                                                                     \
  _Pragma("unroll") for (int u = 0; u < 28; ++u) {                                                      \
    if (u == 6) lwait(fl, F_P1B, 2 * (l + 1));                                                            \
    if (u == 8) arrive_nr(fl, F_ARR1);                                                                  \
    if (u == QGO) arrive_nr(fl, F_KVD);                                                                 \
    if (u == 14) lwait(fl, F_P1C, 2 * (l + 1));                                                            \
    _Pragma("unroll") for (int t = 0; t < 4; ++t) {                                                     \
      SCHED();                                                                                          \
      acc[4 * (u & 1) + t] = MM;                                                                        \
      SCHED();                                                                                          \
      if (u + 2 < 28) xf[u + 2][t] = P1_X(u + 2, t);                                                    \
    }                                                                                                   \
    if (u >= 15 && u < 21) PRE1(u - 15);                                                                \
    SCHED();                                                                                            \
  }                                                                                                     \
  lwait(fl, F_SRDY + 1, st + 1);
        if (part < 2) {
          P1_LOOP(mfma(w1[u >> 1], xf[u][t], acc[4 * (u & 1) + t]))
          STAMP();
          PRIO(2);
          const f32x4 cs1 = *(const f32x4*)(sm + L_CS + (nt6 * 16 + 4 * g) * 4);
          f32x2 m[8];
#pragma unroll
          for (int t = 0; t < 8; ++t) m[t] = *(const f32x2*)(stp + t * 128 + i * 8);
          float v[32];
#pragma unroll
          for (int k = 0; k < 32; ++k) { v[k] = __builtin_fmaf(-m[k >> 2].x, cs1[k & 3], acc[k >> 2][k & 3]); OPQ(v[k]); }
          SCHED();
#pragma unroll
          for (int k = 0; k < 32; ++k) { v[k] *= m[k >> 2].y; OPQ(v[k]); }
          SCHED();
          const unsigned off = (g >> 1) * 1024 + nt1 * 512 + half_off;
#pragma unroll
          for (int p = 0; p < 4; ++p) {
            if (nt6 == 1 && g == 3) { v[8 * p] = 1.f; v[8 * p + 4] = 1.f; }
            const u32x4 o = pair16(u32x2{pk(v[8 * p], v[8 * p + 1]), pk(v[8 * p + 2], v[8 * p + 3])}, u32x2{pk(v[8 * p + 4], v[8 * p + 5]), pk(v[8 * p + 6], v[8 * p + 7])});
            if (part == 1) sth(o, ws, off, so_kv + p * 4096);
            *(u32x4*)(sm + (part == 0 ? L_Q + p * 2048 : L_OWN + p * 4096) + off) = o;
          }
        } else {
          P1_LOOP(mfma(xf[u][t], w1[u >> 1], acc[4 * (u & 1) + t]))
          STAMP();
          PRIO(2);
          const float c = *(const float*)(sm + L_CS + (nt6 * 16 + i) * 4);
          const unsigned off = 2048 + nt1 * 1024 + l16;
#pragma unroll
          for (int hh = 0; hh < 2; ++hh) {
            f32x4 ma[8];
#pragma unroll
            for (int t = 0; t < 8; ++t) ma[t] = *(const f32x4*)(stp + (4 * hh + (t >> 1)) * 128 + g * 32 + (t & 1) * 16);
            float v[16];
#pragma unroll
            for (int k = 0; k < 16; ++k) { v[k] = __builtin_fmaf(-ma[k >> 1][2 * (k & 1)], c, acc[4 * hh + (k >> 2)][k & 3]); OPQ(v[k]); }
            SCHED();
#pragma unroll
            for (int k = 0; k < 16; ++k) { v[k] *= ma[k >> 1][2 * (k & 1) + 1]; OPQ(v[k]); }
            SCHED();
#pragma unroll
            for (int p = 0; p < 2; ++p) {
              u32x4 o = {pk(v[8 * p], v[8 * p + 1]), pk(v[8 * p + 2], v[8 * p + 3]), pk(v[8 * p + 4], v[8 * p + 5]), pk(v[8 * p + 6], v[8 * p + 7])};
              if (nt1 == 1 && i == 12) o = u32x4{0x3f803f80u, 0x3f803f80u, 0x3f803f80u, 0x3f803f80u};
              sth(o, ws, off, so_kv + (2 * hh + p) * 4096);
              *(u32x4*)(sm + L_OWN + (2 * hh + p) * 4096 + off) = o;
            }
          }
        }
        STAMP();
        if (wave < 4) {
          WAIT_VM(0);
          gadd(ws, CNT_KV + ((l * 16 + h1) * 16 + c1) * 128);
          STAMP();
        }
      } else {
        if (l) {
          PRIO(3);
          lwait(fl, F_X2, 1);
          dma_rounds<2>(ws, smb, OFF_X + 2 * X_BYTES + c1 * 112 * KB + l16, 0, 4);
          WAIT_VM(16); arrive_nr(fl, F_P1A);
          dma_rounds<2>(ws, smb, OFF_X + 2 * X_BYTES + c1 * 112 * KB + l16, 4, 7);
        } else {
          WAIT_VM(40); arrive_nr(fl, F_P1A);
        }
        WAIT_VM(24); arrive_nr(fl, F_P1B);
        WAIT_VM(0); arrive_nr(fl, F_P1C);
        PRE34(0, 6)
      }
      PRIO(0);
    }
    BAR();
    {
      IDS();
      STAMP();
      const int T = 8 * c1 + wave;
      const u32x4 q = lds16(sm + L_Q + wave * 1024 + l16);
      const char* kv = sm + L_HALO + (wave >> 1) * 4096 + l16;
      const u32x4 o = attn_tile(kv, q, wave, lane, [&](int k) { PRE1(6 + k); });
      sth(o, ws, l16, OFF_O + l * O_BYTES + (T * 16 + h1) * KB);
      STAMP();
      WAIT_VM(0);
      gadd(ws, CNT_O + (l * 128 + T) * 128);
      STAMP();
      arrive_nr(fl, F_GO2);
      if (wave < 7) { lwait(fl, F_GO2, 8 * (l + 1)); PRE34(28, 36) } else { undef<8>(&w4[12]); }
      if (wave == 7) {
        PRIO(3);
        const int P = 4 * c3 + (m3 >> 2);
        gpoll(ws, CNT_O + (l * 128 + 2 * P) * 128, CNT_O + (l * 128 + 2 * P + 1) * 128, 16);
        STAMP();
        const unsigned src = OFF_O + l * O_BYTES + P * 32 * KB + l16;
#pragma unroll
        for (int k = 0; k < 4; ++k) dma8k(ws, src + k * 8192, smb + L_REG + k * 8192);
        dma8k(ws, OFF_X + 2 * l * X_BYTES + (P * 28 + 7 * (m3 & 3)) * KB + l16, smb + L_RES);
        WAIT_VM(0);
      }
    }
    BAR();
    if (wave0 == 7) {
      IDS();
      serve_x(ws, sm, smb, fl, 2 * l + 1, c1, CNT_X1 + (l * 64 + 4 * c1) * 128, 4, F_ARR3, F_ARR3, 7 * (l + 1), st + 4, lane);
      STAMP();
      if (act5) {
        const unsigned cH = CNT_H + (l * 16 + c3) * 4 * 128;
        const int P = 4 * c3 + th;
        const unsigned rsrc = OFF_X + (2 * l + 1) * X_BYTES + (P * 28 + 4 * ng) * KB + f32_off;
        lwait(fl, F_ARR4, 7 * (l + 1));
        __builtin_amdgcn_s_sleep(16);
        gpoll(ws, cH, cH + 128, 8);
        fl[F_RDY] = st + 5;
        lwait(fl, F_GO4, l + 1);
        STAMP();
        __builtin_amdgcn_s_sleep(8);
        gpoll(ws, cH + 256, cH + 384, 8);
        fl[F_RDY + 1] = st + 5;
        STAMP();
        dma4k(ws, rsrc, smb + L_RES);
        dma4k(ws, rsrc + 56 * KB, smb + L_RES + 4096);
        WAIT_VM(0);
        arrive_nr(fl, F_RED);
      }
      undef<14>(w1);
    } else {
      {
        IDS();
        STAMP();
        const char* os = sm + L_REG + l16;
        u32x4 oa[16], ob[16];
#pragma unroll
        for (int s = 0; s < 6; ++s) { oa[s] = lds16(os + s * 1024); ob[s] = lds16(os + (16 + s) * 1024); }
        const u32x2 xa = *(const u32x2*)(sm + L_RES + wave * 1024 + res_off), xb = *(const u32x2*)(sm + L_RES + wave * 1024 + 512 + res_off);
        f32x4 a0 = {0.f, 0.f, 0.f, 0.f}, a1 = {0.f, 0.f, 0.f, 0.f};
#pragma unroll
        for (int s = 0; s < 16; ++s) {
          SCHED();
          a0 = mfma(w3[s], oa[s], a0);
          SCHED();
          if (s + 6 < 16) oa[s + 6] = lds16(os + (s + 6) * 1024);
          SCHED();
          a1 = mfma(w3[s], ob[s], a1);
          SCHED();
          if (s + 6 < 16) ob[s + 6] = lds16(os + (22 + s) * 1024);
          if (s < 8) PRE1(36 + s);
        }
        PRIO(2);
        float v[8] = {a0[0] + bflo(xa.x), a0[1] + bfhi(xa.x), a0[2] + bflo(xa.y), a0[3] + bfhi(xa.y),
                      a1[0] + bflo(xb.x), a1[1] + bfhi(xb.x), a1[2] + bflo(xb.y), a1[3] + bfhi(xb.y)};
#pragma unroll
        for (int k = 0; k < 8; ++k) OPQ(v[k]);
        const int P = 4 * c3 + (m3 >> 2);
        sth(pair16(u32x2{pk(v[0], v[1]), pk(v[2], v[3])}, u32x2{pk(v[4], v[5]), pk(v[6], v[7])}), ws, l16,
            OFF_X + (2 * l + 1) * X_BYTES + (P * 28 + J3) * KB);
        float s1a = v[0] + v[1], s1b = v[4] + v[5], s2a = v[0] * v[0], s2b = v[4] * v[4];
        OPQ(s1a); OPQ(s1b); OPQ(s2a); OPQ(s2b);
        s1a += v[2]; s1b += v[6]; s2a = __builtin_fmaf(v[1], v[1], s2a); s2b = __builtin_fmaf(v[5], v[5], s2b);
        OPQ(s1a); OPQ(s1b); OPQ(s2a); OPQ(s2b);
        s1a += v[3]; s1b += v[7]; s2a = __builtin_fmaf(v[2], v[2], s2a); s2b = __builtin_fmaf(v[6], v[6], s2b);
        OPQ(s2a); OPQ(s2b);
        s2a = __builtin_fmaf(v[3], v[3], s2a); s2b = __builtin_fmaf(v[7], v[7], s2b);
        auto r1 = __builtin_amdgcn_permlane32_swap(f2u(s1a), f2u(s1b), false, false);
        auto r2 = __builtin_amdgcn_permlane32_swap(f2u(s2a), f2u(s2b), false, false);
        float s1 = u2f(r1[0]) + u2f(r1[1]), s2 = u2f(r2[0]) + u2f(r2[1]);
        OPQ(s1); OPQ(s2);
        auto t = __builtin_amdgcn_permlane16_swap(f2u(s1), f2u(s2), false, false);
        sth32(f2u(u2f(t[0]) + u2f(t[1])), ws, (g >> 1) * 3584 + (lane & 31) * 4, OFF_ST + (2 * l + 1) * ST_BYTES + (2 * P * 28 + J3) * 128);
        STAMP();
        WAIT_VM(0);
        if (arrive(fl, F_ARR3, 7 * (l + 1))) { gadd(ws, CNT_X1 + (l * 64 + P) * 128); arrive_nr(fl, F_GO3); }
        lwait(fl, F_GO3, l + 1);
        PRE34(44, 52)

        PRIO(0);
        STAMP();
      }
      u32x4 hf[2][16];
      auto half4 = [&](auto x_t) {
        constexpr int X = decltype(x_t)::value;
        IDS();
        const char* xs = sm + X * L_REG + f32_off;
        const char* stp = sm + L_ST + X * 512 + (lane & 31) * 8;
        const char* csp = sm + L_CS + (96 + wave * 16 + 4 * (lane >> 5)) * 4;
        f32x16 a0, a1;
        u32x4 xa[28], xb[28];
        f32x2 m0, m1; f32x4 cg0, cg1, cu0, cu1;
        unsigned sfl;
        lwait(fl, F_RDY + X, st + 4);
        STAMP();
        PRIO(1 - X);
#define P4_LOOP(STREAM)                                                                                 \
  _Pragma("unroll") for (int r = 0; r < 16; ++r) { a0[r] = 0.f; a1[r] = 0.f; }                          \
  _Pragma("unroll") for (int s = 0; s < 3; ++s) { xa[s] = lds16(xs + s * 1024); xb[s] = lds16(xs + (28 + s) * 1024); } \
  _Pragma("unroll") for (int s = 0; s < 28; ++s) {                                                      \
    if (s == 25) { sfl = fl[F_SRDY + X]; asm volatile("" ::: "memory"); m0 = *(const f32x2*)stp; m1 = *(const f32x2*)(stp + 256); } \
    if (s == 26) { cg0 = *(const f32x4*)csp; cg1 = *(const f32x4*)(csp + 32); }                         \
    if (s == 27) { cu0 = *(const f32x4*)(csp + 448); cu1 = *(const f32x4*)(csp + 480); }                \
    if (X == 0 && s >= 16 && (s & 1) == 0) PRE1(44 + (s >> 1));                                         \
    SCHED();                                                                                            \
    a0 = mfma32(w4[s], xa[s], a0);                                                                      \
    SCHED();                                                                                            \
    if (s + 3 < 28) xa[s + 3] = lds16(xs + (s + 3) * 1024);                                             \
    SCHED();                                                                                            \
    a1 = mfma32(w4[s], xb[s], a1);                                                                      \
    SCHED();                                                                                            \
    if (s + 3 < 28) xb[s + 3] = lds16(xs + (31 + s) * 1024);                                            \
    if (X == 1 && s == 4 && wave < 4) {                                                                 \
      WAIT_VM(0);                                                                                       \
      if (arrive(fl, F_ARR4, 7 * (l + 1))) gadd(ws, CNT_H + ((l * 16 + c1) * 4 + (h1 >> 3)) * 128);                 \
    }                                                                                                   \
    if (X == 1 && s >= 5 && s < 23) w5[9 + s] = ldwf(rs, s < YS ? l16 : vo5, so5, 9 + s);               \
    SCHED();                                                                                            \
  }
        const unsigned so5 = OFF_WD + ((act5 ? ng : 0) * 112 + 16 * wave) * 2 * KB, vo5 = opaque_v(l16 + (wave < 4 ? 0u : 0x40000000u));
        P4_LOOP(0)
        if ((unsigned)__builtin_amdgcn_readfirstlane(sfl) < st + 4) {
          lwait(fl, F_SRDY + X, st + 4);
          m0 = *(const f32x2*)stp; m1 = *(const f32x2*)(stp + 256);
        }
        STAMP();
        PRIO(2);
        const unsigned so = OFF_H + l * H_BYTES + ((4 * c1 + 2 * X) * 112 + J4) * KB;
        sth(geglu8(a0, m0, cg0, cg1, cu0, cu1), ws, f32_off, so);
        sth(geglu8(a1, m1, cg0, cg1, cu0, cu1), ws, f32_off, so + 112 * KB);
        STAMP();
        SCHED();
        if (X == 0) {
          if (wave >= 4) {
            WAIT_VM(0);
            if (arrive(fl, F_ARR4, 7 * (l + 1))) gadd(ws, CNT_H + ((l * 16 + c1) * 4 + (h1 >> 3)) * 128);
          }
        } else {
          const bool early = wave < 4 && act5 && lready(fl, F_RDY + 0, st + 5);
          if (!early) {
            WAIT_VM(0);
            if (arrive(fl, F_ARR4 + 1, 7 * (l + 1))) { gadd(ws, CNT_H + ((l * 16 + c1) * 4 + 2 + (h1 >> 3)) * 128); arrive_nr(fl, F_GO4); }
            lwait(fl, F_GO4, l + 1);
            if (wave >= 4) { PRE34(53 + YS, 76) }
            if (act5) lwait(fl, F_RDY + 0, st + 5);
          }
          SCHED();
          const unsigned soh = act5 ? OFF_H + l * H_BYTES + ((4 * c3 + th) * 112 + 16 * wave) * KB : OFF_WD;
#pragma unroll
          for (int s = 0; s < 16; ++s) hf[0][s] = ldhf(rs, f32_off, soh, s);
          SCHED();
          if (early) {
            WAIT_VM(16);
            if (arrive(fl, F_ARR4 + 1, 7 * (l + 1))) { gadd(ws, CNT_H + ((l * 16 + c1) * 4 + 2 + (h1 >> 3)) * 128); arrive_nr(fl, F_GO4); }
          }
          PRIO(0);
          STAMP();
        }
      };
      half4(std::integral_constant<int, 0>{});
      half4(std::integral_constant<int, 1>{});
      SCHED();
      auto mma5 = [&](auto x_t) {
        constexpr int X = decltype(x_t)::value;
        IDS();
        const unsigned soh = OFF_H + l * H_BYTES + ((4 * c3 + 2 + th) * 112 + 16 * wave) * KB;
        f32x16 a0, a1;
#pragma unroll
        for (int r = 0; r < 16; ++r) { a0[r] = 0.f; a1[r] = 0.f; }
#pragma unroll
        for (int s = 0; s < 16; ++s) {
          SCHED();
          a0 = mfma32(w5[2 * s], hf[X][s], a0); a1 = mfma32(w5[2 * s + 1], hf[X][s], a1);
          SCHED();
          if (X == 0) hf[1][s] = ldhf(rs, f32_off, soh, s);
        }
        SCHED();
        char* mine = sm + X * L_REG + wave * 8192 + l16;
#pragma unroll
        for (int k = 0; k < 4; ++k) {
          *(f32x4*)(mine + k * 1024) = f32x4{a0[4 * k], a0[4 * k + 1], a0[4 * k + 2], a0[4 * k + 3]};
          *(f32x4*)(mine + (4 + k) * 1024) = f32x4{a1[4 * k], a1[4 * k + 1], a1[4 * k + 2], a1[4 * k + 3]};
        }
        if (X == 1) arrive_nr(fl, F_RED);
      };
      if (act5) {
        lwait(fl, F_RDY + 1, st + 5);
        STAMP();
        PRIO(1);
        mma5(std::integral_constant<int, 0>{});
        PRIO(0);
        STAMP();
        mma5(std::integral_constant<int, 1>{});
        STAMP();
      }
      SCHED();
      if (l == 0 && wave0 < 6) {
        IDS();
#pragma unroll
        for (int s = 0; s < 14; ++s) w1[s] = ldwf(rs, l16, OFF_WQKV + (h1 * 6 + nt6) * 14 * KB, s);
      } else {
        undef<14>(w1);
      }
      SCHED();
    }
    {
      IDS();
      if (act5) {
        if (wave < 7) PRIO(2);
        lwait(fl, F_RED, 8 * (l + 1));
        fin5(ws, out, sm, wave >> 2, wave & 3, 4 * c3 + 2 * (wave >> 2) + th, 4 * ng + (wave & 3), l, lane);
        STAMP();
        if (l == 0) {
          WAIT_VM(0);
          if (arrive(fl, F_ARR5, 8)) gadd(ws, CNT_X2 + c3 * 256 + th * 128);
        }
        if (wave < 7) PRIO(0);
        STAMP();
      }
    }
  }
}

extern "C" void kernel_launch(void* const* d_in, const int* in_sizes, int n_in, void* d_out, int out_size, void* d_ws,
                              size_t ws_size, hipStream_t stream) {
  const uint16_t* x = (const uint16_t*)d_in[0];
  const uint16_t* wqkv = (const uint16_t*)d_in[1];
  const uint16_t* wo = (const uint16_t*)d_in[2];
  const uint16_t* wg = (const uint16_t*)d_in[4];
  const uint16_t* wu = (const uint16_t*)d_in[5];
  const uint16_t* wd = (const uint16_t*)d_in[6];
  char* ws = (char*)d_ws;
  (void)hipFuncSetAttribute((const void*)k_main, hipFuncAttributeMaxDynamicSharedMemorySize, L_BYTES);
  k_prep<<<256, NT, 0, stream>>>(x, wqkv, wo, wg, wu, wd, ws);
  k_main<<<256, NT, L_BYTES, stream>>>(ws, (uint16_t*)d_out);
}
```

```cpp
#include <hip/hip_runtime.h>
#include <hip/hip_bf16.h>
#include <stdint.h>
#include <type_traits>

typedef short bf16x8 __attribute__((ext_vector_type(8)));
typedef float f32x4 __attribute__((ext_vector_type(4)));
typedef float f32x2 __attribute__((ext_vector_type(2)));
typedef float f32x16 __attribute__((ext_vector_type(16)));
typedef unsigned u32x4 __attribute__((ext_vector_type(4)));
typedef unsigned u32x2 __attribute__((ext_vector_type(2)));
typedef __bf16 bf16x2_t __attribute__((ext_vector_type(2)));
#define GAS __attribute__((address_space(1)))
#define LAS __attribute__((address_space(3)))
typedef volatile LAS unsigned* lflag;

constexpr int D_ = 448, DH = 28, F_ = 1792;
constexpr int NT = 512;
constexpr unsigned KB = 1024;

constexpr unsigned CNT_X2 = 0;
constexpr unsigned CNT_O  = CNT_X2 + 32 * 128;
constexpr unsigned CNT_X1 = CNT_O + 256 * 128;
constexpr unsigned CNT_H  = CNT_X1 + 128 * 128;
constexpr unsigned CNT_KV = CNT_H + 128 * 128;
constexpr unsigned NCTRL  = CNT_KV / 128 + 512;
static_assert(NCTRL <= 1280, "k_prep clears five lines per workgroup");
constexpr unsigned OFF_WQKV = 160 * KB;
constexpr unsigned OFF_WO   = OFF_WQKV + 16 * 6 * 14 * KB;
constexpr unsigned OFF_WGU  = OFF_WO + 28 * 16 * KB;
constexpr unsigned OFF_WD   = OFF_WGU + 112 * 28 * KB;
constexpr unsigned OFF_CS   = OFF_WD + 7 * 56 * 4 * KB;
constexpr unsigned X_BYTES  = 128 * 28 * 512;
constexpr unsigned OFF_X    = OFF_CS + 32 * KB;
constexpr unsigned ST_BYTES = 128 * 28 * 128;
constexpr unsigned OFF_ST   = OFF_X + 5 * X_BYTES;
constexpr unsigned KV_BYTES = 16 * 64 * 4 * KB;
constexpr unsigned OFF_KV   = OFF_ST + 4 * ST_BYTES;
constexpr unsigned O_BYTES  = 128 * 16 * KB;
constexpr unsigned OFF_O    = OFF_KV + 2 * KV_BYTES;
constexpr unsigned H_BYTES  = 128 * 56 * KB;
constexpr unsigned OFF_H    = OFF_O + 2 * O_BYTES;
constexpr unsigned WS_USED  = OFF_H + 2 * H_BYTES;

constexpr int L_REG = 80 * 1024;
constexpr int L_HALO = 24 * 1024, L_OWN = 56 * 1024, L_Q = 72 * 1024;
constexpr int L_RES = 136 * 1024;
constexpr int L_ST = L_RES + 8192;
constexpr int L_CS = L_ST + 1024;
constexpr int L_FL = L_CS + 2048;
constexpr int L_BYTES = L_FL + 256;
enum { F_RDY = 0, F_SRDY = 2, F_ARR1 = 4, F_ARR3 = 5, F_ARR4 = 6, F_ARR5 = 8, F_GO4 = 9, F_P1B = 10, F_P1C = 11, F_X2 = 12, F_KVD = 13 };
constexpr int p1_off(int r, int p) { return (r < 2 ? 24 + 16 * r : r == 2 ? 0 : r < 6 ? 80 + 16 * (r - 3) : p < 2 ? 16 : 120) * 1024 + p * 4096; }

__device__ __forceinline__ f32x4 mfma(u32x4 a, u32x4 b, f32x4 c) {
  return __builtin_amdgcn_mfma_f32_16x16x32_bf16(__builtin_bit_cast(bf16x8, a), __builtin_bit_cast(bf16x8, b), c, 0, 0, 0);
}
__device__ __forceinline__ f32x16 mfma32(u32x4 a, u32x4 b, f32x16 c) {
  return __builtin_amdgcn_mfma_f32_32x32x16_bf16(__builtin_bit_cast(bf16x8, a), __builtin_bit_cast(bf16x8, b), c, 0, 0, 0);
}
__device__ __forceinline__ unsigned pk(float a, float b) {
  bf16x2_t v = {(__bf16)a, (__bf16)b};
  return __builtin_bit_cast(unsigned, v);
}
__device__ __forceinline__ unsigned pks(float a, float b) {
  unsigned r;
  asm("v_cvt_pk_bf16_f32 %0, %1, %2" : "=v"(r) : "v"(a), "v"(b));
  return r;
}
__device__ __forceinline__ float bflo(unsigned u) { return __builtin_bit_cast(float, u << 16); }
__device__ __forceinline__ float bfhi(unsigned u) { return __builtin_bit_cast(float, u & 0xffff0000u); }
__device__ __forceinline__ unsigned f2u(float f) { return __builtin_bit_cast(unsigned, f); }
__device__ __forceinline__ float u2f(unsigned u) { return __builtin_bit_cast(float, u); }
__device__ __forceinline__ u32x4 lds16(const char* p) { return *(const u32x4*)p; }
constexpr int YS = 17;
constexpr int QGO = 20;
#define SCHED() __builtin_amdgcn_sched_barrier(0)
#define OPQ(x) asm("" : "+v"(x))
#define PRIO(n) __builtin_amdgcn_s_setprio(n)
#define WAIT_VM(n) asm volatile("s_waitcnt vmcnt(" #n ")" ::: "memory")
typedef __amdgpu_buffer_rsrc_t rsrc_t;
__device__ __forceinline__ u32x4 ldw(rsrc_t rs, unsigned voff, unsigned soff) { return __builtin_amdgcn_raw_buffer_load_b128(rs, voff, soff, 0); }
__device__ __forceinline__ u32x4 ldh(rsrc_t rs, unsigned voff, unsigned soff) { return __builtin_amdgcn_raw_buffer_load_b128(rs, voff, soff, 16); }
__device__ __forceinline__ u32x4 ldwf(rsrc_t rs, unsigned l16, unsigned base, int k) { return ldw(rs, l16 + (k & 3) * 1024, base + (k >> 2) * 4096); }
__device__ __forceinline__ u32x4 ldhf(rsrc_t rs, unsigned l16, unsigned base, int k) { return ldh(rs, l16 + (k & 3) * 1024, base + (k >> 2) * 4096); }
__device__ __forceinline__ unsigned opaque(unsigned v) { asm volatile("" : "+s"(v)); return v; }
template <int N> __device__ __forceinline__ void touch(const u32x4* w) {
#pragma unroll
  for (int k = 0; k < N; ++k) asm volatile("" :: "v"(w[k]));
}
template <int N> __device__ __forceinline__ void undef(u32x4* w) {
#pragma unroll
  for (int k = 0; k < N; ++k) asm volatile("" : "=v"(w[k]));
}
__device__ __forceinline__ int opaque_v(int v) { asm volatile("" : "+v"(v)); return v; }
__device__ __forceinline__ void sth(u32x4 v, char* ws, unsigned voff, unsigned soff) {
  asm volatile("global_store_dwordx4 %0, %1, %2 sc1\n\ts_nop 1" :: "v"(voff + soff), "v"(v), "s"(ws) : "memory");
}
__device__ __forceinline__ void sth32(unsigned v, char* ws, unsigned voff, unsigned soff) {
  asm volatile("global_store_dword %0, %1, %2 sc1" :: "v"(voff + soff), "v"(v), "s"(ws) : "memory");
}

typedef short s16x4 __attribute__((ext_vector_type(4)));
__device__ __forceinline__ u32x2 ldtr(const char* p) {
  return __builtin_bit_cast(u32x2, __builtin_amdgcn_ds_read_tr16_b64_v4i16((LAS s16x4*)(LAS char*)p));
}
__device__ __forceinline__ float rbf(float v) { return bflo(pk(v, v) & 0xffffu); }
__device__ __forceinline__ float sum8(u32x4 v) {
  return ((bflo(v.x) + bfhi(v.x)) + (bflo(v.y) + bfhi(v.y))) + ((bflo(v.z) + bfhi(v.z)) + (bflo(v.w) + bfhi(v.w)));
}
__device__ __forceinline__ float dpp_add(float v, const int ctrl) {
  switch (ctrl) {
    case 0: return v + u2f(__builtin_amdgcn_update_dpp(0u, f2u(v), 0xB1, 0xf, 0xf, false));
    case 1: return v + u2f(__builtin_amdgcn_update_dpp(0u, f2u(v), 0x4E, 0xf, 0xf, false));
    case 2: return v + u2f(__builtin_amdgcn_update_dpp(0u, f2u(v), 0x124, 0xf, 0xf, false));
    default: return v + u2f(__builtin_amdgcn_update_dpp(0u, f2u(v), 0x128, 0xf, 0xf, false));
  }
}
__device__ __forceinline__ float sum32(float v) { auto r = __builtin_amdgcn_permlane32_swap(f2u(v), f2u(v), false, false); return u2f(r[0]) + u2f(r[1]); }
__device__ __forceinline__ float sum16(float v) { auto r = __builtin_amdgcn_permlane16_swap(f2u(v), f2u(v), false, false); return u2f(r[0]) + u2f(r[1]); }
__device__ __forceinline__ float wave_sum(float v) { return sum32(sum16(dpp_add(dpp_add(dpp_add(dpp_add(v, 0), 1), 2), 3))); }

constexpr int PW_STRIDE = 80, PW_BYTES = 452 * PW_STRIDE, PX_OFF = PW_BYTES, PR_OFF = PX_OFF + 2 * 7168;
__global__ __launch_bounds__(NT) void k_prep(const uint16_t* __restrict__ x, const uint16_t* __restrict__ wqkv,
                                             const uint16_t* __restrict__ wo, const uint16_t* __restrict__ wg,
                                             const uint16_t* __restrict__ wu, const uint16_t* __restrict__ wd, char* ws) {
  __shared__ __attribute__((aligned(16))) char sm[PR_OFF + 8 * 32 * 4];
  const int b = blockIdx.x, tid = threadIdx.x, lane = tid & 63, wave = __builtin_amdgcn_readfirstlane(tid >> 6);
  const u32x4 xv = *(const u32x4*)(x + (size_t)(b * 8 + wave) * D_ + min(lane, 55) * 8);
  const int vj = (b >> 4) * 16 + (b & 7) * 2 + ((b >> 3) & 1);
  const bool jq = vj < 48, jo = !jq && vj < 62, jg = vj >= 62 && vj < 174, jd = vj >= 174 && vj < 230;
  const int qh = vj & 15, qp = vj >> 4, oj = vj - 48;
  const int gj = 2 * ((vj - 62) >> 2) + (vj & 1), gu = ((vj - 62) >> 1) & 1;
  const int dj = 2 * ((vj - 174) >> 3) + (vj & 1), dq = ((vj - 174) >> 1) & 3;
  const uint16_t* base = jq ? wqkv : jg ? (gu ? wu : wg) : jd ? wd : wo;
  const int eoff = jq ? qp * D_ + DH * qh : jg ? 32 * gj : jd ? dq * 448 * D_ + 32 * dj : jo ? 32 * oj : 0;
  const int ld = jq ? 3 * D_ : jg ? F_ : D_;
  const int c4 = tid & 7;
  const bool cok = !(jq && c4 == 7);
  u32x2 wv[7];
  {
    const char* src = (const char*)(base + eoff);
    const unsigned vo = ((tid >> 3) * ld + (cok ? c4 * 4 : 0)) * 2;
#pragma unroll
    for (int u = 0; u < 7; ++u) wv[u] = *(const GAS u32x2*)(src + (size_t)(u * 128 * ld) + vo);
  }
  unsigned code_word;
  {
    const char* pc; int delta;
    asm volatile("s_getpc_b64 %0\n1:\n\ts_mov_b32 %1, _Z6k_mainPcPt-1b" : "=s"(pc), "=s"(delta));
    code_word = *(const GAS unsigned*)(pc + delta + ((b >> 3) * 8 + (tid & 7)) * 128);
  }
  if (tid < 5) sth32(0u, ws, (b * 5 + tid) * 128, 0u);
  {
    const float e[8] = {bflo(xv.x), bfhi(xv.x), bflo(xv.y), bfhi(xv.y), bflo(xv.z), bfhi(xv.z), bflo(xv.w), bfhi(xv.w)};
    const float mean = wave_sum(lane < 56 ? sum8(xv) : 0.f) * (1.f / D_);
    float q = 0.f;
#pragma unroll
    for (int k = 0; k < 8; ++k) q += (e[k] - mean) * (e[k] - mean);
    const float var = wave_sum(lane < 56 ? q : 0.f) * (1.f / D_);
    const float mb = rbf(mean), sd = rbf(__builtin_sqrtf(rbf(rbf(var) + 1e-5f)));
    const float isd = 1.f / sd;
    float y[8];
#pragma unroll
    for (int k = 0; k < 8; ++k) y[k] = rbf(e[k] - mb) * isd;
    if (lane < 56) {
      char* d = sm + PX_OFF + (lane >> 1) * 256 + wave * 16 + (lane & 1) * 8;
      *(u32x2*)d = u32x2{xv.x, xv.y}; *(u32x2*)(d + 128) = u32x2{xv.z, xv.w};
      *(u32x2*)(d + 7168) = u32x2{pk(y[0], y[1]), pk(y[2], y[3])}; *(u32x2*)(d + 7168 + 128) = u32x2{pk(y[4], y[5]), pk(y[6], y[7])};
    }
  }
#pragma unroll
  for (int u = 0; u < 7; ++u) *(u32x2*)(sm + (u * 64 + (tid >> 3)) * PW_STRIDE + c4 * 8) = cok ? wv[u] : u32x2{0u, 0u};
  if (tid < 40) *(u32x2*)(sm + 448 * PW_STRIDE + tid * 8) = u32x2{0u, 0u};
  asm volatile("" :: "v"(code_word));
  __syncthreads();
  const int i = lane & 15, g = lane >> 4;
  const bool t32 = jg || jd;
  const int s0 = t32 ? wave : wave >> 1, nt = t32 ? (g & 1) : (wave & 1);
  const int r0 = t32 ? 16 * s0 + 4 * (g >> 1) : jo ? DH * s0 + 4 * g : 32 * s0 + 16 * (g >> 1) + 4 * (g & 1);
  const bool zr = jo && g == 3;
  const int r1 = zr ? 448 : r0 + (jo ? 16 : 8), rstep = (jo ? 112 : 128) * PW_STRIDE;
  const int cofs = (i >> 2) * PW_STRIDE + nt * 32 + (i & 3) * 8;
  const bool last = jo || wave < 4;
  u32x4 o[4];
#pragma unroll
  for (int k = 0; k < 4; ++k) {
    const int kk = (k < 3 || last) ? k : 0;
    const u32x2 lo = ldtr(sm + r0 * PW_STRIDE + cofs + kk * rstep), hi = ldtr(sm + r1 * PW_STRIDE + cofs + (zr ? 0 : kk * rstep));
    o[k] = u32x4{lo.x, lo.y, hi.x, hi.y};
  }
  const u32x4 xo = lds16(sm + PX_OFF + min(tid, 447) * 16), yo = lds16(sm + PX_OFF + 7168 + min(tid, 447) * 16);
  if (vj < 230) {
    const unsigned dst = jq ? OFF_WQKV + (qh * 3 + qp) * 28 * KB : jo ? OFF_WO + 2 * oj * 16 * KB : jg ? OFF_WGU + 2 * gj * 28 * KB + gu * 256
                                                                                                   : OFF_WD + ((dj >> 1) * 112 + dq * 28) * 2 * KB + (dj & 1) * KB;
    const unsigned s_stride = jd ? 2048 : 1024, nt_stride = jq ? 14 * 1024 : jo ? 16 * 1024 : jg ? 28 * 1024 : 256;
    const unsigned vo = s0 * s_stride + nt * nt_stride + (t32 ? i * 16 + (g >> 1) * 512 : lane * 16), kstep = (t32 ? 8 : 4) * s_stride;
#pragma unroll
    for (int k = 0; k < 3; ++k) sth(o[k], ws, vo, dst + k * kstep);
    if (last) sth(o[3], ws, vo, dst + 3 * kstep);
  }
  if (tid < 448) {
    const unsigned xoff = ((b >> 2) * 28 + (tid >> 4)) * KB + ((b >> 1) & 1) * 512 + ((tid >> 3) & 1) * 256 + (b & 1) * 128 + (tid & 7) * 16;
    sth(yo, ws, xoff, OFF_X + 4 * X_BYTES);
    sth(xo, ws, xoff, OFF_X);
  }
  if (jq || jg) {
    float csum = sum32((sum8(o[0]) + sum8(o[1])) + (sum8(o[2]) + (last ? sum8(o[3]) : 0.f)));
    if (jq) csum = sum16(csum);
    float* red = (float*)(sm + PR_OFF);
    if (lane < 32) red[wave * 32 + lane] = (jq && (lane >> 4) != (wave & 1)) ? 0.f : csum;
    __syncthreads();
    if (tid < 32) {
      float t = 0.f;
#pragma unroll
      for (int w = 0; w < 8; ++w) t += red[w * 32 + tid];
      const int f = (jq ? qh * 112 + qp * 32 : 32 * gj) + tid;
      ((float*)(ws + OFF_CS))[(f / 112) * 512 + (jq ? 0 : 96 + 112 * gu) + f % 112] = t;
    }
  }
}

__device__ __forceinline__ void lwait(lflag fl, int idx, unsigned tgt) {
  unsigned v, sv, n = 1u << 20;
  const unsigned addr = (unsigned)(uintptr_t)(fl + idx);
  asm volatile("1:\n\tds_read_b32 %0, %3\n\ts_waitcnt lgkmcnt(0)\n\tv_readfirstlane_b32 %1, %0\n\ts_cmp_ge_u32 %1, %4\n\t"
               "s_cbranch_scc1 2f\n\ts_sleep 1\n\ts_sub_u32 %2, %2, 1\n\ts_cbranch_scc0 1b\n2:"
               : "=&v"(v), "=&s"(sv), "+s"(n) : "v"(addr), "s"(tgt) : "memory", "scc");
}
__device__ __forceinline__ bool lready(lflag fl, int idx, unsigned tgt) {
  bool r = (unsigned)__builtin_amdgcn_readfirstlane(fl[idx]) >= tgt;
  asm volatile("" ::: "memory");
  return r;
}
__device__ __forceinline__ bool arrive(lflag fl, int idx, unsigned tgt) {
  unsigned old, so;
  const unsigned addr = (unsigned)(uintptr_t)(fl + idx), one = 1u;
  asm volatile("s_mov_b64 exec, 1\n\tds_add_rtn_u32 %0, %2, %3\n\ts_mov_b64 exec, -1\n\ts_waitcnt lgkmcnt(0)\n\tv_readfirstlane_b32 %1, %0"
               : "=&v"(old), "=s"(so) : "v"(addr), "v"(one) : "memory");
  return so + 1u == tgt;
}
__device__ __forceinline__ void arrive_nr(lflag fl, int idx) {
  const unsigned addr = (unsigned)(uintptr_t)(fl + idx), one = 1u;
  asm volatile("s_mov_b64 exec, 1\n\tds_add_u32 %0, %1\n\ts_mov_b64 exec, -1" :: "v"(addr), "v"(one) : "memory");
}
__device__ __forceinline__ void gadd(char* ws, unsigned off) {
  const unsigned one = 1u;
  asm volatile("s_mov_b64 exec, 1\n\tglobal_atomic_add %0, %1, %2\n\ts_mov_b64 exec, -1" :: "v"(off), "v"(one), "s"(ws) : "memory");
}
__device__ __forceinline__ void gpoll(char* ws, unsigned off0, unsigned off1, unsigned tgt) {
  unsigned a, c, n = 1u << 18;
  off0 = __builtin_amdgcn_readfirstlane(off0); off1 = __builtin_amdgcn_readfirstlane(off1);
  asm volatile("s_nop 4\n1:\n\ts_load_dword %0, %3, %4 glc\n\ts_load_dword %1, %3, %5 glc\n\ts_waitcnt lgkmcnt(0)\n\ts_min_u32 %0, %0, %1\n\t"
               "s_cmp_ge_u32 %0, %6\n\ts_cbranch_scc1 2f\n\ts_sub_u32 %2, %2, 1\n\ts_cbranch_scc0 1b\n2:"
               : "=&s"(a), "=&s"(c), "+s"(n) : "s"(ws), "s"(off0), "s"(off1), "s"(tgt) : "memory", "scc");
}
__device__ __forceinline__ bool gready(char* ws, unsigned off, unsigned tgt) {
  unsigned a;
  off = __builtin_amdgcn_readfirstlane(off);
  asm volatile("s_nop 4\n\ts_load_dword %0, %1, %2 glc\n\ts_waitcnt lgkmcnt(0)" : "=&s"(a) : "s"(ws), "s"(off) : "memory");
  return a >= tgt;
}
#define BAR() do { asm volatile("s_waitcnt lgkmcnt(0)" ::: "memory"); __builtin_amdgcn_s_barrier(); asm volatile("" ::: "memory"); } while (0)
__device__ __forceinline__ void dma8k(const char* ws, unsigned voff, unsigned lds) {
  unsigned keep;
  lds = __builtin_amdgcn_readfirstlane(lds + 4096);
  asm volatile("s_mov_b32 %0, m0\n\ts_mov_b32 m0, %3\n\ts_nop 0\n\t"
               "global_load_lds_dwordx4 %1, %2 offset:-4096 sc1\n\tglobal_load_lds_dwordx4 %1, %2 offset:-3072 sc1\n\t"
               "global_load_lds_dwordx4 %1, %2 offset:-2048 sc1\n\tglobal_load_lds_dwordx4 %1, %2 offset:-1024 sc1\n\t"
               "global_load_lds_dwordx4 %1, %2 sc1\n\tglobal_load_lds_dwordx4 %1, %2 offset:1024 sc1\n\t"
               "global_load_lds_dwordx4 %1, %2 offset:2048 sc1\n\tglobal_load_lds_dwordx4 %1, %2 offset:3072 sc1\n\t"
               "s_mov_b32 m0, %0"
               : "=&s"(keep) : "v"(voff + 4096), "s"(ws), "s"(lds) : "memory");
}
__device__ __forceinline__ void dma4k(const char* ws, unsigned voff, unsigned lds) {
  unsigned keep;
  lds = __builtin_amdgcn_readfirstlane(lds);
  asm volatile("s_mov_b32 %0, m0\n\ts_mov_b32 m0, %3\n\ts_nop 0\n\t"
               "global_load_lds_dwordx4 %1, %2 sc1\n\tglobal_load_lds_dwordx4 %1, %2 offset:1024 sc1\n\t"
               "global_load_lds_dwordx4 %1, %2 offset:2048 sc1\n\tglobal_load_lds_dwordx4 %1, %2 offset:3072 sc1\n\t"
               "s_mov_b32 m0, %0"
               : "=&s"(keep) : "v"(voff), "s"(ws), "s"(lds) : "memory");
}
__device__ __forceinline__ void dma1k(const char* ws, unsigned voff, unsigned lds) {
  unsigned keep;
  lds = __builtin_amdgcn_readfirstlane(lds);
  asm volatile("s_mov_b32 %0, m0\n\ts_mov_b32 m0, %3\n\ts_nop 0\n\tglobal_load_lds_dwordx4 %1, %2 sc1\n\ts_mov_b32 m0, %0"
               : "=&s"(keep) : "v"(voff), "s"(ws), "s"(lds) : "memory");
}

constexpr float GC1 = -2.f * 0.7978845608028654f * 1.4426950408889634f, GC2 = GC1 * 0.044715f;
__device__ __forceinline__ u32x4 pair16(u32x2 a, u32x2 b) {
  auto rx = __builtin_amdgcn_permlane32_swap(a.x, b.x, false, false);
  auto ry = __builtin_amdgcn_permlane32_swap(a.y, b.y, false, false);
  return u32x4{rx[0], ry[0], rx[1], ry[1]};
}
__device__ __forceinline__ float allg_max(float m) {
  auto r = __builtin_amdgcn_permlane32_swap(f2u(m), f2u(m), false, false);
  m = fmaxf(u2f(r[0]), u2f(r[1]));
  auto q = __builtin_amdgcn_permlane16_swap(f2u(m), f2u(m), false, false);
  return fmaxf(u2f(q[0]), u2f(q[1]));
}

__device__ __forceinline__ void dma_x(char* ws, unsigned smb, int xi, int T0, int X, int lane) {
  const unsigned src = OFF_X + xi * X_BYTES + (unsigned)T0 * 14 * KB + lane * 16;
#pragma unroll
  for (int k = 0; k < 7; ++k) dma8k(ws, src + k * 8192, smb + X * L_REG + k * 8192);
}
__device__ __forceinline__ void stat_loads(char* ws, u32x4 (&p)[14], int xi, int T0, int lane) {
  const unsigned vo = (lane >> 4) * 3584 + (lane & 15) * 16 + OFF_ST + xi * ST_BYTES + (unsigned)T0 * 3584;
  asm volatile("global_load_dwordx4 %0, %14, %15 sc1\n\tglobal_load_dwordx4 %1, %14, %15 offset:256 sc1\n\t"
               "global_load_dwordx4 %2, %14, %15 offset:512 sc1\n\tglobal_load_dwordx4 %3, %14, %15 offset:768 sc1\n\t"
               "global_load_dwordx4 %4, %14, %15 offset:1024 sc1\n\tglobal_load_dwordx4 %5, %14, %15 offset:1280 sc1\n\t"
               "global_load_dwordx4 %6, %14, %15 offset:1536 sc1\n\tglobal_load_dwordx4 %7, %14, %15 offset:1792 sc1\n\t"
               "global_load_dwordx4 %8, %14, %15 offset:2048 sc1\n\tglobal_load_dwordx4 %9, %14, %15 offset:2304 sc1\n\t"
               "global_load_dwordx4 %10, %14, %15 offset:2560 sc1\n\tglobal_load_dwordx4 %11, %14, %15 offset:2816 sc1\n\t"
               "global_load_dwordx4 %12, %14, %15 offset:3072 sc1\n\tglobal_load_dwordx4 %13, %14, %15 offset:3328 sc1"
               : "=&v"(p[0]), "=&v"(p[1]), "=&v"(p[2]), "=&v"(p[3]), "=&v"(p[4]), "=&v"(p[5]), "=&v"(p[6]), "=&v"(p[7]), "=&v"(p[8]),
                 "=&v"(p[9]), "=&v"(p[10]), "=&v"(p[11]), "=&v"(p[12]), "=&v"(p[13])
               : "v"(vo), "s"(ws) : "memory");
}
__device__ __forceinline__ bool stat_wait_poll(char* ws, u32x4 (&p)[14], unsigned off, unsigned tgt) {
  unsigned v;
  asm volatile("global_load_dword %0, %15, %16 sc1\n\ts_waitcnt vmcnt(0)"
               : "=&v"(v), "+v"(p[0]), "+v"(p[1]), "+v"(p[2]), "+v"(p[3]), "+v"(p[4]), "+v"(p[5]), "+v"(p[6]), "+v"(p[7]), "+v"(p[8]), "+v"(p[9]),
                 "+v"(p[10]), "+v"(p[11]), "+v"(p[12]), "+v"(p[13]) : "v"(off), "s"(ws) : "memory");
  return __all(v >= tgt);
}
__device__ __forceinline__ void stat_landed(u32x4 (&p)[14]) {
  asm volatile("" : "+v"(p[0]), "+v"(p[1]), "+v"(p[2]), "+v"(p[3]), "+v"(p[4]), "+v"(p[5]), "+v"(p[6]), "+v"(p[7]), "+v"(p[8]), "+v"(p[9]),
                    "+v"(p[10]), "+v"(p[11]), "+v"(p[12]), "+v"(p[13]) :: "memory");
}
template <int P0> __device__ __forceinline__ void dma_rounds(char* ws, unsigned smb, unsigned src, int r0, int r1) {
#pragma unroll
  for (int r = r0; r < r1; ++r)
#pragma unroll
    for (int p = P0; p < P0 + 2; ++p) dma4k(ws, src + p * 28 * KB + r * 4096, smb + p1_off(r, p));
}
__device__ __forceinline__ void stat_finish(char* sm, lflag fl, const u32x4 (&p)[14], int X, unsigned stage, int lane) {
  float a[4];
#pragma unroll
  for (int r = 0; r < 4; ++r) {
    float t0 = u2f(p[0][r]) + u2f(p[1][r]), t1 = u2f(p[2][r]) + u2f(p[3][r]);
    OPQ(t0); OPQ(t1);
#pragma unroll
    for (int k = 4; k < 14; k += 2) { t0 += u2f(p[k][r]); t1 += u2f(p[k + 1][r]); OPQ(t0); OPQ(t1); }
    a[r] = t0 + t1;
  }
  float ms[4], rsd[4];
#pragma unroll
  for (int r = 0; r < 4; ++r) {
    float s1 = a[r] + u2f(__builtin_amdgcn_update_dpp(0u, f2u(a[r]), 0x128, 0xf, 0xf, false));
    float s2 = u2f(__builtin_amdgcn_update_dpp(0u, f2u(s1), 0x124, 0xf, 0xf, false));
    float mean = s1 * (1.f / D_);
    OPQ(mean);
    float var = fmaxf(__builtin_fmaf(-mean, mean, s2 * (1.f / D_)), 0.f);
    ms[r] = mean; rsd[r] = __builtin_amdgcn_rsqf(var + 1e-5f);
  }
  if ((lane & 15) < 4) {
    char* d = sm + L_ST + X * 512 + ((lane >> 4) * 16 + (lane & 3) * 4) * 8;
    *(f32x4*)d = f32x4{ms[0], rsd[0], ms[1], rsd[1]};
    *(f32x4*)(d + 16) = f32x4{ms[2], rsd[2], ms[3], rsd[3]};
  }
  asm volatile("" ::: "memory");
  fl[F_SRDY + X] = stage;
}

__device__ __forceinline__ void serve_x(char* ws, char* sm, unsigned smb, lflag fl, int xi, int c, unsigned cnt, unsigned tgt, unsigned stage, int lane) {
  u32x4 p[14];
  gpoll(ws, cnt, cnt + 128, tgt);
  dma_x(ws, smb, xi, 8 * c, 0, lane);
  stat_loads(ws, p, xi, 8 * c, lane);
  WAIT_VM(14);
  BAR();
  const bool now = stat_wait_poll(ws, p, cnt + (2 + (lane & 1)) * 128, tgt);
  if (now) dma_x(ws, smb, xi, 8 * c + 4, 1, lane);
  stat_finish(sm, fl, p, 0, stage, lane);
  if (!now) {
    gpoll(ws, cnt + 256, cnt + 384, tgt);
    dma_x(ws, smb, xi, 8 * c + 4, 1, lane);
  }
  stat_loads(ws, p, xi, 8 * c + 4, lane);
  WAIT_VM(14);
  fl[F_RDY + 1] = stage;
  (void)stat_wait_poll(ws, p, cnt, 0u);
  stat_finish(sm, fl, p, 1, stage, lane);
}

template <class PRE>
__device__ __forceinline__ u32x4 attn_tile(const char* kv, u32x4 q, int qt, int lane, u32x4 first, PRE pre) {
  const int i = lane & 15, g = lane >> 4;
  f32x4 s[18];
  u32x4 kf[18];
#pragma unroll
  for (int j = 0; j < 6; ++j) kf[j] = lds16(kv + (j >> 1) * 4096 + (j & 1) * 1024);
#pragma unroll
  for (int j = 0; j < 18; ++j) {
    if (j + 6 < 18) kf[j + 6] = lds16(kv + ((j + 6) >> 1) * 4096 + (j & 1) * 1024);
    SCHED();
    s[j] = mfma(kf[j], q, f32x4{0.f, 0.f, 0.f, 0.f});
    if (j % 2 == 1) pre(j / 2);
    SCHED();
  }
  u32x4 vf[18];
#pragma unroll
  for (int j = 0; j < 4; ++j) vf[j] = lds16(kv + (j >> 1) * 4096 + 2048 + (j & 1) * 1024);
  const int dq = 16 * (qt & 1) + i - 4 * g;
#pragma unroll
  for (int u = 0; u < 2; ++u)
#pragma unroll
    for (int r = 0; r < 4; ++r) s[u][r] = dq < 16 * u + r ? s[u][r] : s[16 + u][r];
  float mx[4];
#pragma unroll
  for (int r = 0; r < 4; ++r) mx[r] = fmaxf(s[2][r], s[3][r]);
#pragma unroll
  for (int j = 4; j < 18; j += 2)
#pragma unroll
    for (int r = 0; r < 4; ++r) mx[r] = fmaxf(fmaxf(mx[r], s[j % 16][r]), s[(j + 1) % 16][r]);
  const float m = allg_max(fmaxf(fmaxf(mx[0], mx[1]), fmaxf(mx[2], mx[3])));
  constexpr float SC = 0.18898223650461361f * 1.4426950408889634f;
  const float mc = -m * SC;
  const f32x4 m4 = {mc, mc, mc, mc}, c4 = {SC, SC, SC, SC};
  SCHED();
#pragma unroll
  for (int j = 0; j < 16; ++j) s[j] = __builtin_elementwise_fma(s[j], c4, m4);
  pre(9);
  SCHED();
  f32x4 o0 = {0.f, 0.f, 0.f, 0.f}, o1 = {0.f, 0.f, 0.f, 0.f};
  u32x4 plast;
#pragma unroll
  for (int j = 0; j < 9; ++j) {
    if (j + 2 < 9) { vf[2 * j + 4] = lds16(kv + (j + 2) * 4096 + 2048); vf[2 * j + 5] = lds16(kv + (j + 2) * 4096 + 3072); }
    u32x4 pf;
    if (j < 8) {
      float p[8];
#pragma unroll
      for (int k = 0; k < 8; ++k) p[k] = __builtin_amdgcn_exp2f(s[2 * j + (k >> 2)][k & 3]);
      pf = u32x4{pk(p[0], p[1]), pk(p[2], p[3]), pk(p[4], p[5]), pk(p[6], p[7])};
      if (j == 0) { plast = pf & ~first; pf &= first; }
    } else {
      pf = plast;
    }
    o0 = mfma(vf[2 * j], pf, o0);
    o1 = mfma(vf[2 * j + 1], pf, o1);
    if (j < 6) { pre(10 + 2 * j); pre(11 + 2 * j); }
    SCHED();
  }
  auto r = __builtin_amdgcn_permlane32_swap(f2u(o1[0]), f2u(o1[0]), false, false);
  auto t = __builtin_amdgcn_permlane16_swap(r[1], r[1], false, false);
  const float inv = __builtin_amdgcn_rcpf(u2f(t[1]));
  return u32x4{pk(o0[0] * inv, o0[1] * inv), pk(o0[2] * inv, o0[3] * inv), pk(o1[0] * inv, o1[1] * inv), pk(o1[2] * inv, o1[3] * inv)};
}

__device__ __forceinline__ u32x4 geglu8(const f32x16& acc, f32x2 ms, const f32x4& cg0, const f32x4& cg1, const f32x4& cu0, const f32x4& cu1) {
  const float q = ms.y * ms.y, k1 = GC1 * ms.y, k2 = GC2 * ms.y * q, ir = __builtin_amdgcn_rcpf(q), nm = -ms.x;
  float a[8], b[8], t[8];
#pragma unroll
  for (int r = 0; r < 8; ++r) { a[r] = __builtin_fmaf(nm, r < 4 ? cg0[r & 3] : cg1[r & 3], acc[r]); OPQ(a[r]); }
  SCHED();
#pragma unroll
  for (int r = 0; r < 8; ++r) { t[r] = a[r] * a[r]; OPQ(t[r]); }
  SCHED();
#pragma unroll
  for (int r = 0; r < 8; ++r) { t[r] = __builtin_fmaf(t[r], k2, k1); OPQ(t[r]); }
  SCHED();
#pragma unroll
  for (int r = 0; r < 8; ++r) { t[r] = t[r] * a[r]; OPQ(t[r]); }
  SCHED();
#pragma unroll
  for (int r = 0; r < 8; ++r) {
    t[r] = __builtin_amdgcn_exp2f(t[r]);
    b[r] = __builtin_fmaf(nm, r < 4 ? cu0[r & 3] : cu1[r & 3], acc[r + 8]); OPQ(b[r]);
  }
  SCHED();
#pragma unroll
  for (int r = 0; r < 8; ++r) { t[r] = __builtin_fmaf(t[r], ir, ir); OPQ(t[r]); }
  SCHED();
#pragma unroll
  for (int r = 0; r < 8; ++r) { t[r] = __builtin_amdgcn_rcpf(t[r]); b[r] = a[r] * b[r]; OPQ(b[r]); }
  SCHED();
#pragma unroll
  for (int r = 0; r < 8; ++r) { t[r] = t[r] * b[r]; OPQ(t[r]); }
  SCHED();
  return u32x4{pks(t[0], t[1]), pks(t[2], t[3]), pks(t[4], t[5]), pks(t[6], t[7])};
}

__device__ __forceinline__ void fin5(char* ws, uint16_t* out, char* sm, int X, int nt, int P, int J, int l, int lane) {
  const int i = lane & 15, g = lane >> 4;
  const u32x4 xr = lds16(sm + L_RES + X * 4096 + nt * 1024 + lane * 16);
  const char* red = sm + X * L_REG + nt * 2048 + lane * 16;
  f32x4 pa[7], pb[7];
#pragma unroll
  for (int k = 0; k < 7; ++k) { pa[k] = *(const f32x4*)(red + k * 8192); pb[k] = *(const f32x4*)(red + k * 8192 + 1024); }
  float v[8] = {bflo(xr.x), bfhi(xr.x), bflo(xr.y), bfhi(xr.y), bflo(xr.z), bfhi(xr.z), bflo(xr.w), bfhi(xr.w)};
  SCHED();
#pragma unroll
  for (int k = 0; k < 7; ++k) {
#pragma unroll
    for (int r = 0; r < 8; ++r) { v[r] += r < 4 ? pa[k][r] : pb[k][r - 4]; OPQ(v[r]); }
    SCHED();
  }
  const u32x4 o = {pk(v[0], v[1]), pk(v[2], v[3]), pk(v[4], v[5]), pk(v[6], v[7])};
  if (l == 0) {
    sth(o, ws, (g & 1) * 512 + (i + 16 * (g >> 1)) * 16, OFF_X + 2 * X_BYTES + (P * 28 + J) * KB);
    float s1 = v[0] + v[1], s2 = v[0] * v[0], s3 = v[2] + v[3], s4 = v[1] * v[1];
    OPQ(s1); OPQ(s2); OPQ(s3); OPQ(s4);
    s1 += v[4]; s3 += v[5]; s2 = __builtin_fmaf(v[2], v[2], s2); s4 = __builtin_fmaf(v[3], v[3], s4);
    OPQ(s1); OPQ(s2); OPQ(s3); OPQ(s4);
    s1 += v[6]; s3 += v[7]; s2 = __builtin_fmaf(v[4], v[4], s2); s4 = __builtin_fmaf(v[5], v[5], s4);
    OPQ(s1); OPQ(s2); OPQ(s3); OPQ(s4);
    s1 += s3; s2 = __builtin_fmaf(v[6], v[6], s2); s4 = __builtin_fmaf(v[7], v[7], s4);
    OPQ(s1); OPQ(s2); OPQ(s4);
    s2 += s4;
    auto r = __builtin_amdgcn_permlane32_swap(f2u(s1), f2u(s2), false, false);
    const float sv = u2f(r[0]) + u2f(r[1]);
    sth32(f2u(sv), ws, (g & 1) * 3584 + (lane >> 5) * 64 + i * 4, OFF_ST + 2 * ST_BYTES + (2 * P * 28 + J) * 128);
  } else {
    sth(pair16(u32x2{o.x, o.y}, u32x2{o.z, o.w}), (char*)out, ((P * 32 + (lane & 31)) * D_ + 16 * J + 8 * (lane >> 5)) * 2, 0u);
  }
}

#define IDS()                                                                                                   \
  const int lane = opaque_v(lane0), wave = opaque(wave0), b = opaque(b0), xcd = b & 7, rk = b >> 3;             \
  const int c1 = 4 * (xcd >> 1) + (rk & 3), h1 = 8 * (xcd & 1) + (rk >> 2), c3 = 2 * xcd + (rk & 1), u5 = rk >> 1; \
  const int m3 = (u5 & 3) * 4 + (u5 >> 2);                                                                      \
  const bool act5 = u5 < 14;                                                                                    \
  const int th = u5 & 1, ng = u5 >> 1, i = lane & 15, g = lane >> 4;                                            \
  const int part = wave < 6 ? 2 - (wave >> 1) : 0, nt1 = wave & 1, nt6 = 2 * part + nt1, w7 = min(wave, 6), J4 = 7 * h1 + w7, J3 = 7 * (m3 & 3) + w7; \
  const unsigned l16 = lane * 16, half_off = (lane & 31) * 16, res_off = (i + 16 * (g & 1)) * 16 + 8 * (g >> 1), f32_off = (g & 1) * 512 + (i + 16 * (g >> 1)) * 16; \
  (void)c1; (void)h1; (void)c3; (void)act5; (void)ng; (void)th; (void)part; (void)nt1; (void)J4; (void)J3; (void)l16; (void)half_off; (void)res_off; (void)f32_off; (void)nt6
__global__ __launch_bounds__(NT, 2) void k_main(char* ws, uint16_t* out) {
  extern __shared__ __attribute__((aligned(16))) char sm[];
  const int tid = threadIdx.x, lane0 = tid & 63, wave0 = __builtin_amdgcn_readfirstlane(tid >> 6), b0 = blockIdx.x;
  IDS();
  lflag fl = (lflag)(LAS char*)(sm + L_FL);
  const rsrc_t rs = __builtin_amdgcn_make_buffer_rsrc(ws, 0, WS_USED, 0x00020000);
  const unsigned smb = (unsigned)(uintptr_t)(LAS char*)sm;
  u32x4 w1[14];
  if (wave0 == 7) {
    PRIO(3);
    const unsigned src = OFF_X + 4 * X_BYTES + c1 * 112 * KB + l16;
    dma1k(ws, OFF_CS + h1 * 2048 + l16, smb + L_CS);
    dma1k(ws, OFF_CS + h1 * 2048 + 1024 + l16, smb + L_CS + 1024);
    dma_rounds<0>(ws, smb, src, 0, 7);
    fl[lane] = 0u;
    *(f32x2*)(sm + L_ST + lane * 8) = f32x2{0.f, 1.f};
    *(f32x2*)(sm + L_ST + 512 + lane * 8) = f32x2{0.f, 1.f};
    undef<14>(w1);
  } else if (wave0 < 6) {
#pragma unroll
    for (int s = 0; s < 14; ++s) w1[s] = ldwf(rs, l16, OFF_WQKV + (h1 * 6 + nt6) * 14 * KB, s);
  } else {
    dma_rounds<2>(ws, smb, OFF_X + 4 * X_BYTES + c1 * 112 * KB + l16, 0, 7);
    undef<14>(w1);
  }
  BAR();

#pragma unroll 1
  for (int l = 0; l < 2; ++l) {
    const unsigned st = 5 * l;
    u32x4 w3[16], w4[28], w5[32];
#define PRE1(k)                                                                                         \
  do {                                                                                                  \
    if ((k) < 16) w3[(k)] = ldwf(rs, l16, OFF_WO + J3 * 16 * KB, (k));                                  \
    else if ((k) < 44) w4[(k) - 16] = ldwf(rs, l16, OFF_WGU + J4 * 28 * KB, (k) - 16);                  \
    else w5[(k) - 44] = ldwf(rs, l16, OFF_WD + ((act5 ? ng : 0) * 112 + 16 * w7) * 2 * KB, (k) - 44);   \
  } while (0)
#define PRE34(k0, k1)                                                                                   \
  SCHED();                                                                                              \
  _Pragma("unroll") for (int k = k0; k < k1; ++k) PRE1(k);                                              \
  SCHED();
    if (wave0 == 7) {
      IDS();
      if (l == 0) {
        fl[F_SRDY] = 1u; fl[F_SRDY + 1] = 1u;
        WAIT_VM(40); BAR();
        WAIT_VM(24); arrive_nr(fl, F_P1B);
        WAIT_VM(0); arrive_nr(fl, F_P1C);
      } else {
        const unsigned src = OFF_X + 2 * X_BYTES + c1 * 112 * KB + l16;
        u32x4 pa[14], pb[14];
        if (act5) {
          lwait(fl, F_ARR5, 8);
          __builtin_amdgcn_s_sleep(6);
        } else {
          lwait(fl, F_GO4, 1);
#pragma unroll 1
          for (int k = 0; k < 64 && !gready(ws, CNT_X2 + c1 * 256, 1); ++k) __builtin_amdgcn_s_sleep(24);
        }
        gpoll(ws, CNT_X2 + c1 * 256, CNT_X2 + c1 * 256 + 128, 7);
        fl[F_X2] = 1u;
        dma_rounds<0>(ws, smb, src, 0, 4);
        WAIT_VM(16); BAR();
        dma_rounds<0>(ws, smb, src, 4, 7);
        WAIT_VM(24); arrive_nr(fl, F_P1B);
        stat_loads(ws, pa, 2, 8 * c1, lane);
        stat_loads(ws, pb, 2, 8 * c1 + 4, lane);
        WAIT_VM(28); arrive_nr(fl, F_P1C);
        WAIT_VM(14);
        stat_landed(pa);
        stat_finish(sm, fl, pa, 0, st + 1, lane);
        WAIT_VM(0);
        stat_landed(pb);
        stat_finish(sm, fl, pb, 1, st + 1, lane);
      }
      lwait(fl, F_ARR1, 6 * (l + 1));
#pragma unroll 1
      for (int k = 0; k < 8 - 4 * c1; ++k) {
        const u32x4 kz = {0u, 0u, g == 3 ? 0xff80u : 0u, 0u}, vz = {0u, 0u, 0u, 0u};
        char* d = sm + L_HALO + k * 4096 + l16;
        *(u32x4*)d = kz; *(u32x4*)(d + 1024) = kz; *(u32x4*)(d + 2048) = vz; *(u32x4*)(d + 3072) = vz;
      }
      if (c1 > 0) gpoll(ws, CNT_KV + ((l * 16 + h1) * 16 + max(c1 - 2, 0)) * 128, CNT_KV + ((l * 16 + h1) * 16 + c1 - 1) * 128, 4);
      const unsigned src = OFF_KV + l * KV_BYTES + (h1 * 64 + 4 * c1 - 8) * 4096 + l16;
#pragma unroll
      for (int k = 0; k < 4; ++k) if (2 * c1 + k >= 4) dma8k(ws, src + k * 8192, smb + L_HALO + k * 8192);
      WAIT_VM(0);
      PRIO(0);
      undef<6>(w3);
    } else {
      if (wave0 < 6) touch<14>(w1);
      IDS();
      if (wave < 6) {
        BAR();
        if (wave >= 4) { __builtin_amdgcn_s_sleep(24); lwait(fl, F_KVD, 6 * l + 4); }
        const char* xs = sm + (g >> 1) * 1024 + half_off;
        const char* xs2 = sm + opaque_v(L_REG + (g >> 1) * 1024 + half_off);
        const char* stp = sm + L_ST;
        const unsigned so_kv = OFF_KV + l * KV_BYTES + (h1 * 64 + 4 * c1) * 4096;
        f32x4 acc[8];
        u32x4 xf[28][4];
#pragma unroll
        for (int t = 0; t < 8; ++t) acc[t] = f32x4{0.f, 0.f, 0.f, 0.f};
#define P1_O(u, t) (p1_off((u) >> 2, 2 * ((u) & 1) + ((t) >> 1)) + (((u) >> 1) & 1) * 2048 + ((t) & 1) * 512)
#define P1_X(u, t) lds16(P1_O(u, t) < L_REG ? xs + P1_O(u, t) : xs2 + (P1_O(u, t) - L_REG))
#define P1_LOOP(MM)                                                                                     \
  _Pragma("unroll") for (int u = 0; u < 2; ++u) {                                                       \
    _Pragma("unroll") for (int t = 0; t < 4; ++t) xf[u][t] = P1_X(u, t);                                \
  }                                                                                                     \
  _Pragma("unroll") for (int u = 0; u < 28; ++u) {                                                      \
    if (u == 6) lwait(fl, F_P1B, 2 * (l + 1));                                                            \
    if (u == 8) arrive_nr(fl, F_ARR1);                                                                  \
    if (u == QGO) arrive_nr(fl, F_KVD);                                                                 \
    if (u == 14) lwait(fl, F_P1C, 2 * (l + 1));                                                            \
    _Pragma("unroll") for (int t = 0; t < 4; ++t) {                                                     \
      SCHED();                                                                                          \
      acc[4 * (u & 1) + t] = MM;                                                                        \
      SCHED();                                                                                          \
      if (u + 2 < 28) xf[u + 2][t] = P1_X(u + 2, t);                                                    \
    }                                                                                                   \
    if (u >= 15 && u < 21) PRE1(u - 15);                                                                \
    SCHED();                                                                                            \
  }                                                                                                     \
  lwait(fl, F_SRDY + 1, st + 1);
        if (part < 2) {
          P1_LOOP(mfma(w1[u >> 1], xf[u][t], acc[4 * (u & 1) + t]))
          PRIO(2);
          const f32x4 cs1 = *(const f32x4*)(sm + L_CS + (nt6 * 16 + 4 * g) * 4);
          f32x2 m[8];
#pragma unroll
          for (int t = 0; t < 8; ++t) m[t] = *(const f32x2*)(stp + t * 128 + i * 8);
          float v[32];
#pragma unroll
          for (int k = 0; k < 32; ++k) { v[k] = __builtin_fmaf(-m[k >> 2].x, cs1[k & 3], acc[k >> 2][k & 3]); OPQ(v[k]); }
          SCHED();
#pragma unroll
          for (int k = 0; k < 32; ++k) { v[k] *= m[k >> 2].y; OPQ(v[k]); }
          SCHED();
          const unsigned off = (g >> 1) * 1024 + nt1 * 512 + half_off;
#pragma unroll
          for (int p = 0; p < 4; ++p) {
            if (nt6 == 1 && g == 3) { v[8 * p] = 1.f; v[8 * p + 4] = 1.f; }
            const u32x4 o = pair16(u32x2{pk(v[8 * p], v[8 * p + 1]), pk(v[8 * p + 2], v[8 * p + 3])}, u32x2{pk(v[8 * p + 4], v[8 * p + 5]), pk(v[8 * p + 6], v[8 * p + 7])});
            if (part == 1) sth(o, ws, off, so_kv + p * 4096);
            *(u32x4*)(sm + (part == 0 ? L_Q + p * 2048 : L_OWN + p * 4096) + off) = o;
          }
        } else {
          P1_LOOP(mfma(xf[u][t], w1[u >> 1], acc[4 * (u & 1) + t]))
          PRIO(2);
          const float c = *(const float*)(sm + L_CS + (nt6 * 16 + i) * 4);
          const unsigned off = 2048 + nt1 * 1024 + l16;
#pragma unroll
          for (int hh = 0; hh < 2; ++hh) {
            f32x4 ma[8];
#pragma unroll
            for (int t = 0; t < 8; ++t) ma[t] = *(const f32x4*)(stp + (4 * hh + (t >> 1)) * 128 + g * 32 + (t & 1) * 16);
            float v[16];
#pragma unroll
            for (int k = 0; k < 16; ++k) { v[k] = __builtin_fmaf(-ma[k >> 1][2 * (k & 1)], c, acc[4 * hh + (k >> 2)][k & 3]); OPQ(v[k]); }
            SCHED();
#pragma unroll
            for (int k = 0; k < 16; ++k) { v[k] *= ma[k >> 1][2 * (k & 1) + 1]; OPQ(v[k]); }
            SCHED();
#pragma unroll
            for (int p = 0; p < 2; ++p) {
              u32x4 o = {pk(v[8 * p], v[8 * p + 1]), pk(v[8 * p + 2], v[8 * p + 3]), pk(v[8 * p + 4], v[8 * p + 5]), pk(v[8 * p + 6], v[8 * p + 7])};
              if (nt1 == 1 && i == 12) o = u32x4{0x3f803f80u, 0x3f803f80u, 0x3f803f80u, 0x3f803f80u};
              sth(o, ws, off, so_kv + (2 * hh + p) * 4096);
              *(u32x4*)(sm + L_OWN + (2 * hh + p) * 4096 + off) = o;
            }
          }
        }
        if (wave < 4) {
          WAIT_VM(0);
          gadd(ws, CNT_KV + ((l * 16 + h1) * 16 + c1) * 128);
        }
      } else {
        if (l) {
          PRIO(3);
          lwait(fl, F_X2, 1);
          dma_rounds<2>(ws, smb, OFF_X + 2 * X_BYTES + c1 * 112 * KB + l16, 0, 4);
          WAIT_VM(16); BAR();
          dma_rounds<2>(ws, smb, OFF_X + 2 * X_BYTES + c1 * 112 * KB + l16, 4, 7);
        } else {
          WAIT_VM(40); BAR();
        }
        WAIT_VM(24); arrive_nr(fl, F_P1B);
        WAIT_VM(0); arrive_nr(fl, F_P1C);
        PRE34(0, 6)
      }
      PRIO(0);
    }
    {
      IDS();
      u32x4 first;
#pragma unroll
      for (int k = 0; k < 4; ++k) {
        const int t = 16 * (wave & 1) + i - 4 * g - 16 * (k >> 1) - 2 * (k & 1);
        first[k] = t < 0 ? 0xffffffffu : t == 0 ? 0xffff0000u : 0u;
      }
      asm volatile("" : "+v"(first));
      BAR();
      const int T = 8 * c1 + wave;
      const u32x4 q = lds16(sm + L_Q + wave * 1024 + l16);
      const char* kv = sm + L_HALO + (wave >> 1) * 4096 + l16;
      const unsigned lo16 = l16;
      u32x4 o;
      {
        const unsigned l16 = opaque_v(lo16 + (wave == 7 ? 0x40000000u : 0u));
        o = attn_tile(kv, q, wave, lane, first, [&](int k) { PRE1(6 + k); });
      }
      sth(o, ws, l16, OFF_O + l * O_BYTES + (T * 16 + h1) * KB);
      WAIT_VM(0);
      gadd(ws, CNT_O + (l * 128 + T) * 128);
      BAR();
      if (wave < 7) { PRE34(28, 36) } else { undef<8>(&w4[12]); }
      if (wave == 7) {
        PRIO(3);
        const int P = 4 * c3 + (m3 >> 2);
        gpoll(ws, CNT_O + (l * 128 + 2 * P) * 128, CNT_O + (l * 128 + 2 * P + 1) * 128, 16);
        const unsigned src = OFF_O + l * O_BYTES + P * 32 * KB + l16;
#pragma unroll
        for (int k = 0; k < 4; ++k) dma8k(ws, src + k * 8192, smb + L_REG + k * 8192);
        const unsigned rsrc = OFF_X + 2 * l * X_BYTES + (P * 28 + 7 * (m3 & 3)) * KB + l16;
        dma4k(ws, rsrc, smb + L_RES);
        dma4k(ws, rsrc + 3072, smb + L_RES + 3072);
        WAIT_VM(0);
      }
    }
    BAR();
    if (wave0 == 7) {
      IDS();
      BAR();
      serve_x(ws, sm, smb, fl, 2 * l + 1, c1, CNT_X1 + (l * 64 + 4 * c1) * 128, 4, st + 4, lane);
      if (act5) {
        const unsigned cH = CNT_H + (l * 16 + c3) * 4 * 128;
        const int P = 4 * c3 + th;
        const unsigned rsrc = OFF_X + (2 * l + 1) * X_BYTES + (P * 28 + 4 * ng) * KB + f32_off;
        lwait(fl, F_ARR4, 7 * (l + 1));
        __builtin_amdgcn_s_sleep(16);
        gpoll(ws, cH, cH + 128, 8);
        fl[F_RDY] = st + 5;
        lwait(fl, F_GO4, l + 1);
        __builtin_amdgcn_s_sleep(8);
        gpoll(ws, cH + 256, cH + 384, 8);
        fl[F_RDY + 1] = st + 5;
        dma4k(ws, rsrc, smb + L_RES);
        dma4k(ws, rsrc + 56 * KB, smb + L_RES + 4096);
        WAIT_VM(0);
      }
      undef<14>(w1);
    } else {
      {
        IDS();
        const char* os = sm + L_REG + l16;
        u32x4 oa[16], ob[16];
#pragma unroll
        for (int s = 0; s < 6; ++s) { oa[s] = lds16(os + s * 1024); ob[s] = lds16(os + (16 + s) * 1024); }
        const u32x2 xa = *(const u32x2*)(sm + L_RES + wave * 1024 + res_off), xb = *(const u32x2*)(sm + L_RES + wave * 1024 + 512 + res_off);
        f32x4 a0 = {0.f, 0.f, 0.f, 0.f}, a1 = {0.f, 0.f, 0.f, 0.f};
#pragma unroll
        for (int s = 0; s < 16; ++s) {
          SCHED();
          a0 = mfma(w3[s], oa[s], a0);
          SCHED();
          if (s + 6 < 16) oa[s + 6] = lds16(os + (s + 6) * 1024);
          SCHED();
          a1 = mfma(w3[s], ob[s], a1);
          SCHED();
          if (s + 6 < 16) ob[s + 6] = lds16(os + (22 + s) * 1024);
          if (s < 8) PRE1(36 + s);
        }
        PRIO(2);
        float v[8] = {a0[0] + bflo(xa.x), a0[1] + bfhi(xa.x), a0[2] + bflo(xa.y), a0[3] + bfhi(xa.y),
                      a1[0] + bflo(xb.x), a1[1] + bfhi(xb.x), a1[2] + bflo(xb.y), a1[3] + bfhi(xb.y)};
#pragma unroll
        for (int k = 0; k < 8; ++k) OPQ(v[k]);
        const int P = 4 * c3 + (m3 >> 2);
        sth(pair16(u32x2{pk(v[0], v[1]), pk(v[2], v[3])}, u32x2{pk(v[4], v[5]), pk(v[6], v[7])}), ws, l16,
            OFF_X + (2 * l + 1) * X_BYTES + (P * 28 + J3) * KB);
        float s1a = v[0] + v[1], s1b = v[4] + v[5], s2a = v[0] * v[0], s2b = v[4] * v[4];
        OPQ(s1a); OPQ(s1b); OPQ(s2a); OPQ(s2b);
        s1a += v[2]; s1b += v[6]; s2a = __builtin_fmaf(v[1], v[1], s2a); s2b = __builtin_fmaf(v[5], v[5], s2b);
        OPQ(s1a); OPQ(s1b); OPQ(s2a); OPQ(s2b);
        s1a += v[3]; s1b += v[7]; s2a = __builtin_fmaf(v[2], v[2], s2a); s2b = __builtin_fmaf(v[6], v[6], s2b);
        OPQ(s2a); OPQ(s2b);
        s2a = __builtin_fmaf(v[3], v[3], s2a); s2b = __builtin_fmaf(v[7], v[7], s2b);
        auto r1 = __builtin_amdgcn_permlane32_swap(f2u(s1a), f2u(s1b), false, false);
        auto r2 = __builtin_amdgcn_permlane32_swap(f2u(s2a), f2u(s2b), false, false);
        float s1 = u2f(r1[0]) + u2f(r1[1]), s2 = u2f(r2[0]) + u2f(r2[1]);
        OPQ(s1); OPQ(s2);
        auto t = __builtin_amdgcn_permlane16_swap(f2u(s1), f2u(s2), false, false);
        sth32(f2u(u2f(t[0]) + u2f(t[1])), ws, (g >> 1) * 3584 + (lane & 31) * 4, OFF_ST + (2 * l + 1) * ST_BYTES + (2 * P * 28 + J3) * 128);
        WAIT_VM(0);
        if (arrive(fl, F_ARR3, 7 * (l + 1))) gadd(ws, CNT_X1 + (l * 64 + P) * 128);
        BAR();
        PRE34(44, 52)
        PRIO(0);
      }
      u32x4 hf[2][16];
      auto half4 = [&](auto x_t) {
        constexpr int X = decltype(x_t)::value;
        IDS();
        const char* xs = sm + X * L_REG + f32_off;
        const char* stp = sm + L_ST + X * 512 + (lane & 31) * 8;
        const char* csp = sm + L_CS + (96 + wave * 16 + 4 * (lane >> 5)) * 4;
        f32x16 a0, a1;
        u32x4 xa[28], xb[28];
        f32x2 m0, m1; f32x4 cg0, cg1, cu0, cu1;
        unsigned sfl;
        if (X == 0) BAR(); else lwait(fl, F_RDY + X, st + 4);
        if (X == 0 || wave >= 4) PRIO(1); else PRIO(0);
#define P4_LOOP()                                                                                       \
  _Pragma("unroll") for (int r = 0; r < 16; ++r) { a0[r] = 0.f; a1[r] = 0.f; }                          \
  _Pragma("unroll") for (int s = 0; s < 3; ++s) { xa[s] = lds16(xs + s * 1024); xb[s] = lds16(xs + (28 + s) * 1024); } \
  _Pragma("unroll") for (int s = 0; s < 28; ++s) {                                                      \
    if (s == 25) { sfl = fl[F_SRDY + X]; asm volatile("" ::: "memory"); m0 = *(const f32x2*)stp; m1 = *(const f32x2*)(stp + 256); } \
    if (s == 26) { cg0 = *(const f32x4*)csp; cg1 = *(const f32x4*)(csp + 32); }                         \
    if (s == 27) { cu0 = *(const f32x4*)(csp + 448); cu1 = *(const f32x4*)(csp + 480); }                \
    if (X == 0 && s >= 16 && (s & 1) == 0) PRE1(44 + (s >> 1));                                         \
    SCHED();                                                                                            \
    a0 = mfma32(w4[s], xa[s], a0);                                                                      \
    SCHED();                                                                                            \
    if (s + 3 < 28) xa[s + 3] = lds16(xs + (s + 3) * 1024);                                             \
    SCHED();                                                                                            \
    a1 = mfma32(w4[s], xb[s], a1);                                                                      \
    SCHED();                                                                                            \
    if (s + 3 < 28) xb[s + 3] = lds16(xs + (31 + s) * 1024);                                            \
    if (X == 1 && s == 4) {                                                                             \
      WAIT_VM(0);                                                                                       \
      PRIO(0);                                                                                          \
      if (arrive(fl, F_ARR4, 7 * (l + 1))) gadd(ws, CNT_H + ((l * 16 + c1) * 4 + (h1 >> 3)) * 128);                 \
    }                                                                                                   \
    if (X == 1 && s >= 5 && s < 23) w5[9 + s] = ldwf(rs, s < YS ? l16 : vo5, so5, 9 + s);               \
    SCHED();                                                                                            \
  }
        const unsigned so5 = OFF_WD + ((act5 ? ng : 0) * 112 + 16 * wave) * 2 * KB, vo5 = opaque_v(l16 + (wave < 4 ? 0u : 0x40000000u));
        P4_LOOP()
        if ((unsigned)__builtin_amdgcn_readfirstlane(sfl) < st + 4) {
          lwait(fl, F_SRDY + X, st + 4);
          m0 = *(const f32x2*)stp; m1 = *(const f32x2*)(stp + 256);
        }
        PRIO(2);
        const unsigned so = OFF_H + l * H_BYTES + ((4 * c1 + 2 * X) * 112 + J4) * KB;
        sth(geglu8(a0, m0, cg0, cg1, cu0, cu1), ws, f32_off, so);
        sth(geglu8(a1, m1, cg0, cg1, cu0, cu1), ws, f32_off, so + 112 * KB);
        SCHED();
        if (X == 1) {
          const bool early = wave < 4 && act5 && lready(fl, F_RDY + 0, st + 5);
          if (!early) {
            WAIT_VM(0);
            if (arrive(fl, F_ARR4 + 1, 7 * (l + 1))) { gadd(ws, CNT_H + ((l * 16 + c1) * 4 + 2 + (h1 >> 3)) * 128); arrive_nr(fl, F_GO4); }
            lwait(fl, F_GO4, l + 1);
            if (wave >= 4) { PRE34(53 + YS, 76) }
            if (act5) lwait(fl, F_RDY + 0, st + 5);
          }
          SCHED();
          const unsigned soh = act5 ? OFF_H + l * H_BYTES + ((4 * c3 + th) * 112 + 16 * wave) * KB : OFF_WD;
#pragma unroll
          for (int s = 0; s < 16; ++s) hf[0][s] = ldhf(rs, f32_off, soh, s);
          SCHED();
          if (early) {
            WAIT_VM(16);
            if (arrive(fl, F_ARR4 + 1, 7 * (l + 1))) { gadd(ws, CNT_H + ((l * 16 + c1) * 4 + 2 + (h1 >> 3)) * 128); arrive_nr(fl, F_GO4); }
          }
          PRIO(0);
        }
      };
      half4(std::integral_constant<int, 0>{});
      half4(std::integral_constant<int, 1>{});
      SCHED();
      auto mma5 = [&](auto x_t) {
        constexpr int X = decltype(x_t)::value;
        IDS();
        if (X == 1) {
          const unsigned soh = OFF_H + l * H_BYTES + ((4 * c3 + 2 + th) * 112 + 16 * wave) * KB;
          lwait(fl, F_RDY + 1, st + 5);
#pragma unroll
          for (int s = 0; s < 16; ++s) hf[1][s] = ldhf(rs, f32_off, soh, s);
        }
        f32x16 a0, a1;
#pragma unroll
        for (int r = 0; r < 16; ++r) { a0[r] = 0.f; a1[r] = 0.f; }
#pragma unroll
        for (int s = 0; s < 16; ++s) {
          SCHED();
          a0 = mfma32(w5[2 * s], hf[X][s], a0); a1 = mfma32(w5[2 * s + 1], hf[X][s], a1);
          SCHED();
        }
        SCHED();
        char* mine = sm + X * L_REG + wave * 8192 + l16;
#pragma unroll
        for (int k = 0; k < 4; ++k) {
          *(f32x4*)(mine + k * 1024) = f32x4{a0[4 * k], a0[4 * k + 1], a0[4 * k + 2], a0[4 * k + 3]};
          *(f32x4*)(mine + (4 + k) * 1024) = f32x4{a1[4 * k], a1[4 * k + 1], a1[4 * k + 2], a1[4 * k + 3]};
        }
      };
      if (act5) {
        lwait(fl, F_GO4, l + 1);
        PRIO(1);
        mma5(std::integral_constant<int, 0>{});
        PRIO(0);
        mma5(std::integral_constant<int, 1>{});
      }
      SCHED();
      if (l == 0 && wave0 < 6) {
        IDS();
#pragma unroll
        for (int s = 0; s < 14; ++s) w1[s] = ldwf(rs, l16, OFF_WQKV + (h1 * 6 + nt6) * 14 * KB, s);
      } else {
        undef<14>(w1);
      }
      SCHED();
    }
    {
      IDS();
      if (act5) {
        if (wave < 7) PRIO(2);
        BAR();
        fin5(ws, out, sm, wave >> 2, wave & 3, 4 * c3 + 2 * (wave >> 2) + th, 4 * ng + (wave & 3), l, lane);
        if (l == 0) {
          WAIT_VM(0);
          if (arrive(fl, F_ARR5, 8)) gadd(ws, CNT_X2 + c3 * 256 + th * 128);
        }
        if (wave < 7) PRIO(0);
      }
    }
  }
}

extern "C" void kernel_launch(void* const* d_in, const int* in_sizes, int n_in, void* d_out, int out_size, void* d_ws,
                              size_t ws_size, hipStream_t stream) {
  const uint16_t* x = (const uint16_t*)d_in[0];
  const uint16_t* wqkv = (const uint16_t*)d_in[1];
  const uint16_t* wo = (const uint16_t*)d_in[2];
  const uint16_t* wg = (const uint16_t*)d_in[4];
  const uint16_t* wu = (const uint16_t*)d_in[5];
  const uint16_t* wd = (const uint16_t*)d_in[6];
  char* ws = (char*)d_ws;
  (void)hipFuncSetAttribute((const void*)k_main, hipFuncAttributeMaxDynamicSharedMemorySize, L_BYTES);
  k_prep<<<256, NT, 0, stream>>>(x, wqkv, wo, wg, wu, wd, ws);
  k_main<<<256, NT, L_BYTES, stream>>>(ws, (uint16_t*)d_out);
}
```
